```python
import torch

def _softmax(x, axis=-1):
    return torch.softmax(x, dim=axis)
def _gelu(x):
    return torch.nn.functional.gelu(x, approximate="tanh")
def _silu(x):
    return torch.nn.functional.silu(x)
def _sigmoid(x):
    return torch.sigmoid(x)
def _layernorm(x, axis=-1, eps=1e-5):
    m = x.mean(dim=axis, keepdim=True)
    v = x.var(dim=axis, keepdim=True, unbiased=False)
    return (x - m) / torch.sqrt(v + eps)
def _scatter_add(tgt, idx, src, axis=0):
    return tgt.clone().index_add_(axis, idx, src)
def _segment_sum(src, seg, n):
    out = torch.zeros((n,) + src.shape[1:], dtype=src.dtype)
    return out.index_add_(0, seg, src)
def _pad(x, pw):
    flat = tuple(v for pair in reversed(pw) for v in pair)
    return torch.nn.functional.pad(x, flat)

kernel_name = 'sequential_preLN__sliding_block__moe_arch_mod__prompt'

def setup_inputs():

    _w = (lambda a: (lambda t: t.to(torch.bfloat16) if t.is_floating_point() else t)(torch.as_tensor(a)))
    def _A_setup():
        B, S, D = 1, 2048, 640
        return {
            "x": _w((lambda *s: torch.randn(*s, dtype=torch.bfloat16) * 0.05)(B, S, D)),
            "wqkv": _w((lambda *s: torch.randn(*s, dtype=torch.bfloat16) * 0.05)(D, 3 * D)),
            "wo": _w((lambda *s: torch.randn(*s, dtype=torch.bfloat16) * 0.05)(D, D)),
        }
    _A_in = _A_setup()

    def _B_setup():
        B, S, D, F, C = 1, 2048, 640, 1728, 8
        return {
            "x": _w((lambda *s: torch.randn(*s, dtype=torch.bfloat16) * 0.05)(B, S, D)),
            "w_r": _w((lambda *s: torch.randn(*s, dtype=torch.bfloat16) * 0.05)(D)),
            "w_gate": _w((lambda *s: torch.randn(*s, dtype=torch.bfloat16) * 0.05)(D, F)),
            "w_up": _w((lambda *s: torch.randn(*s, dtype=torch.bfloat16) * 0.05)(D, F)),
            "w_down": _w((lambda *s: torch.randn(*s, dtype=torch.bfloat16) * 0.05)(F, D)),
        }
    _B_in = _B_setup()

    return {
        "A_x": _A_in["x"],
        "A_wqkv": _A_in["wqkv"],
        "A_wo": _A_in["wo"],
        "B_x": _B_in["x"],
        "B_w_r": _B_in["w_r"],
        "B_w_gate": _B_in["w_gate"],
        "B_w_up": _B_in["w_up"],
        "B_w_down": _B_in["w_down"],
    }

def reference(A_x, A_wqkv, A_wo, B_x, B_w_r, B_w_gate, B_w_up, B_w_down):
    def _A_ref(x, wqkv, wo):
        B, S, D = x.shape
        H = 32; Dh = D // H; W = 256
        qkv = (lambda x, *d: x.permute(*d))((x @ wqkv).reshape(B, S, 3, H, Dh), 2, 0, 3, 1, 4)
        q, k, v = qkv[0], qkv[1], qkv[2]
        scores = torch.einsum("bhqd,bhkd->bhqk", q, k) * (Dh ** -0.5)
        idx = torch.arange(S)
        mask = (idx[:, None] - idx[None, :] >= 0) & (idx[:, None] - idx[None, :] < W)
        scores = torch.where(mask[None, None], scores, -1e9)
        p = _softmax(scores, axis=-1)
        o = torch.einsum("bhqk,bhkd->bhqd", p, v)
        return (lambda x, *d: x.permute(*d))(o, 0, 2, 1, 3).reshape(B, S, D) @ wo

    def _B_ref(x, w_r, w_gate, w_up, w_down):
        B, S, D = x.shape
        C = 8
        r = x @ w_r
        idx = torch.argsort(-r, axis=-1)[:, :C]
        idx_e = (lambda x, n, axis: torch.repeat_interleave(x, n, dim=axis))(idx.reshape(B, C, 1), D, axis=2)
        x_sel = (lambda x, i, axis: torch.take_along_dim(x, i, dim=axis))(x, idx_e, axis=1)
        g = _silu(x_sel @ w_gate)
        u = x_sel @ w_up
        y = (g * u) @ w_down
        r_sel = _sigmoid((lambda x, i, axis: torch.take_along_dim(x, i, dim=axis))(r, idx, axis=1))
        y = y * r_sel.reshape(B, C, 1)
        b_off = (torch.arange(B) * S).reshape(B, 1)
        flat_idx = (idx + b_off).reshape(B * C)
        zero = (x * 0.0).reshape(B * S, D)
        upd = _scatter_add(zero, flat_idx, y.reshape(B * C, D), axis=0)
        return x + upd.reshape(B, S, D)

    x = A_x
    h = x + _A_ref(_layernorm(x, axis=-1), A_wqkv, A_wo)
    h2 = h + _B_ref(_layernorm(h, axis=-1), B_w_r, B_w_gate, B_w_up, B_w_down)
    return h2
```

```cpp
#include <hip/hip_runtime.h>
#include <hip/hip_bf16.h>
#include <stdint.h>

typedef unsigned short u16;
__device__ __forceinline__ float bf2f(u16 b){ return __uint_as_float(((unsigned)b)<<16); }
__device__ __forceinline__ u16 f2bf(float f){ unsigned u=__float_as_uint(f); u += 0x7FFFu + ((u>>16)&1u); return (u16)(u>>16); }
__device__ __forceinline__ float rbf(float f){ return bf2f(f2bf(f)); }

constexpr int S=2048, D=640, H=32, DH=20, W=256, F=1728, C=8;

__device__ float block_sum(float v, float* sh){
  for(int m=32;m>=1;m>>=1) v += __shfl_xor(v,m);
  int w=threadIdx.x>>6, l=threadIdx.x&63;
  __syncthreads();
  if(l==0) sh[w]=v;
  __syncthreads();
  float t=0; for(int i=0;i<(int)(blockDim.x>>6);++i) t+=sh[i];
  return t;
}

__global__ void k_ln(const u16* in, u16* out){
  __shared__ float sh[16];
  int row=blockIdx.x; const u16* p=in+(size_t)row*D;
  float s=0; for(int i=threadIdx.x;i<D;i+=blockDim.x) s+=bf2f(p[i]);
  float m=block_sum(s,sh)/D;
  float q=0; for(int i=threadIdx.x;i<D;i+=blockDim.x){ float d=bf2f(p[i])-m; q+=d*d; }
  float v=block_sum(q,sh)/D;
  float mb=rbf(m), vb=rbf(v); float sd=rbf(sqrtf(rbf(vb+1e-5f)));
  for(int i=threadIdx.x;i<D;i+=blockDim.x){ out[(size_t)row*D+i]=f2bf(rbf(bf2f(p[i])-mb)/sd); }
}
__global__ void k_gemm(const u16* A, const u16* B, u16* Cc, const u16* res, int M,int N,int K){
  int n=blockIdx.x*blockDim.x+threadIdx.x; int m=blockIdx.y; if(n>=N) return;
  float acc=0; for(int k=0;k<K;++k) acc+=bf2f(A[(size_t)m*K+k])*bf2f(B[(size_t)k*N+n]);
  float r=rbf(acc); if(res) r=rbf(bf2f(res[(size_t)m*N+n])+r);
  Cc[(size_t)m*N+n]=f2bf(r);
}
__global__ void k_attn(const u16* qkv, u16* o){
  int idx=blockIdx.x*blockDim.x+threadIdx.x; if(idx>=S*H) return;
  int h=idx%H, q=idx/H;
  float qv[DH];
  #pragma unroll
  for(int d=0;d<DH;++d) qv[d]=bf2f(qkv[(size_t)q*3*D+h*DH+d]);
  int k0=q-W+1; if(k0<0) k0=0;
  float m=-1e30f;
  const float sc=rbf(0.22360679774997896f);
  for(int k=k0;k<=q;++k){ float s=0;
    #pragma unroll
    for(int d=0;d<DH;++d) s+=qv[d]*bf2f(qkv[(size_t)k*3*D+D+h*DH+d]);
    s=rbf(rbf(s)*0.22360679774997896f); m=fmaxf(m,s);}
  float l=0; float acc[DH];
  #pragma unroll
  for(int d=0;d<DH;++d) acc[d]=0;
  for(int k=k0;k<=q;++k){ float s=0;
    #pragma unroll
    for(int d=0;d<DH;++d) s+=qv[d]*bf2f(qkv[(size_t)k*3*D+D+h*DH+d]);
    s=rbf(rbf(s)*0.22360679774997896f); float p=__expf(s-m); l+=p;
    #pragma unroll
    for(int d=0;d<DH;++d) acc[d]+=p*bf2f(qkv[(size_t)k*3*D+2*D+h*DH+d]); }
  (void)sc;
  #pragma unroll
  for(int d=0;d<DH;++d) o[(size_t)q*D+h*DH+d]=f2bf(acc[d]/l);
}
__global__ void k_router(const u16* hn, const u16* wr, const u16* h, float* r, u16* out){
  __shared__ float sh[16];
  int row=blockIdx.x; float s=0;
  for(int i=threadIdx.x;i<D;i+=blockDim.x){ float x=bf2f(hn[(size_t)row*D+i]); s+=x*bf2f(wr[i]); out[(size_t)row*D+i]=f2bf(bf2f(h[(size_t)row*D+i])+x);}
  float t=block_sum(s,sh); if(threadIdx.x==0) r[row]=rbf(t);
}
__global__ void k_topk(const float* r, int* idx){
  __shared__ float v[S]; __shared__ float bv[256]; __shared__ int bi[256];
  for(int i=threadIdx.x;i<S;i+=256) v[i]=r[i];
  __syncthreads();
  for(int c=0;c<C;++c){
    float b=-INFINITY; int j=-1;
    for(int i=threadIdx.x;i<S;i+=256) if(v[i]>b || j<0){ if(v[i]>b||j<0){b=v[i];j=i;} }
    bv[threadIdx.x]=b; bi[threadIdx.x]=j; __syncthreads();
    if(threadIdx.x==0){ float B=bv[0]; int J=bi[0]; for(int t=1;t<256;++t) if(bv[t]>B || (bv[t]==B && bi[t]<J)){B=bv[t];J=bi[t];} idx[c]=J; v[J]=-INFINITY; }
    __syncthreads();
  }
}
__global__ void k_gateup(const u16* hn, const int* idx, const u16* wg, const u16* wu, u16* act){
  int n=blockIdx.x*blockDim.x+threadIdx.x; int c=blockIdx.y; if(n>=F) return; int t=idx[c];
  float g=0,u=0; for(int k=0;k<D;++k){ float x=bf2f(hn[(size_t)t*D+k]); g+=x*bf2f(wg[(size_t)k*F+n]); u+=x*bf2f(wu[(size_t)k*F+n]); }
  g=rbf(g); u=rbf(u); float sg=rbf(g/(1.f+__expf(-g))); act[c*F+n]=f2bf(sg*u);
}
__global__ void k_down(const u16* act, const int* idx, const float* r, const u16* wd, const u16* hn, const u16* h, u16* out){
  int n=blockIdx.x*blockDim.x+threadIdx.x; int c=blockIdx.y; if(n>=D) return; int t=idx[c];
  float y=0; for(int k=0;k<F;++k) y+=bf2f(act[c*F+k])*bf2f(wd[(size_t)k*D+n]);
  y=rbf(y); float sg=rbf(1.f/(1.f+__expf(-r[t]))); y=rbf(y*sg);
  float b=rbf(bf2f(hn[(size_t)t*D+n])+y); out[(size_t)t*D+n]=f2bf(bf2f(h[(size_t)t*D+n])+b);
}

extern "C" void kernel_launch(void* const* d_in, const int* in_sizes, int n_in, void* d_out, int out_size, void* d_ws, size_t ws_size, hipStream_t stream){
  const u16* x=(const u16*)d_in[0]; const u16* wqkv=(const u16*)d_in[1]; const u16* wo=(const u16*)d_in[2];
  const u16* wr=(const u16*)d_in[4]; const u16* wg=(const u16*)d_in[5]; const u16* wu=(const u16*)d_in[6]; const u16* wd=(const u16*)d_in[7];
  u16* out=(u16*)d_out; char* ws=(char*)d_ws;
  u16* xn=(u16*)ws; ws+=S*D*2; u16* qkv=(u16*)ws; ws+=S*3*D*2; u16* o=(u16*)ws; ws+=S*D*2; u16* h=(u16*)ws; ws+=S*D*2; u16* hn=(u16*)ws; ws+=S*D*2;
  float* r=(float*)ws; ws+=S*4; int* idx=(int*)ws; ws+=256; u16* act=(u16*)ws;
  k_ln<<<S,256,0,stream>>>(x,xn);
  k_gemm<<<dim3((3*D+255)/256,S),256,0,stream>>>(xn,wqkv,qkv,nullptr,S,3*D,D);
  k_attn<<<S*H/64,64,0,stream>>>(qkv,o);
  k_gemm<<<dim3((D+127)/128,S),128,0,stream>>>(o,wo,h,x,S,D,D);
  k_ln<<<S,256,0,stream>>>(h,hn);
  k_router<<<S,256,0,stream>>>(hn,wr,h,r,out);
  k_topk<<<1,256,0,stream>>>(r,idx);
  k_gateup<<<dim3((F+63)/64,C),64,0,stream>>>(hn,idx,wg,wu,act);
  k_down<<<dim3(D/64,C),64,0,stream>>>(act,idx,r,wd,hn,h,out);
}
```

```cpp
#include <hip/hip_runtime.h>
#include <stdint.h>

#define GAS __attribute__((address_space(1)))
#define LAS __attribute__((address_space(3)))
#define DI __device__ __forceinline__
typedef unsigned short u16;
typedef unsigned long long u64;
typedef unsigned v4u __attribute__((ext_vector_type(4)));
typedef unsigned v2u __attribute__((ext_vector_type(2)));
typedef float f32x2 __attribute__((ext_vector_type(2)));
typedef float f32x4 __attribute__((ext_vector_type(4)));
typedef float f32x16 __attribute__((ext_vector_type(16)));
typedef __bf16 bf16x2 __attribute__((ext_vector_type(2)));
typedef __bf16 bf16x4 __attribute__((ext_vector_type(4)));
typedef __bf16 bf16x8 __attribute__((ext_vector_type(8)));
typedef GAS u64 gu64;
typedef GAS unsigned gu32;
typedef const LAS unsigned char* lptr;
typedef __amdgpu_buffer_rsrc_t rsrc_t;
#define RLX_AGENT __ATOMIC_RELAXED, __HIP_MEMORY_SCOPE_AGENT

constexpr int WS_EPOCH = 0;
constexpr int WS_CF = 4096;
constexpr int WS_QF = 8192;
constexpr int WS_OF = 40960;
constexpr int WS_HD = 73728;
constexpr int WS_TS = 77824;
constexpr int WS_CS = 94208;
constexpr int WS_S = 262144;
constexpr int WS_RK = 524288;
constexpr int WS_ACT = 544768;
constexpr int WS_QKV = 1 << 20;
constexpr int WS_OU = 9 << 20;
constexpr int WS_H = 12 << 20;
constexpr int WS_STAMP = 15 << 20;
constexpr int WS_BYTES = 16 << 20;

constexpr int L_MISC = 131072;
constexpr int L_K = 0, L_V = 30720, L_ONE = 61440;
constexpr int L_WO = 0, L_RED = 102400, L_SG = 122880, L_ST = 131072, L_KEY = 132096;
constexpr int LDS_BYTES = 147456;

constexpr float QSCALE = 0.22360679774997896f * 1.4426950408889634f;
constexpr unsigned SENT = 0xFFFFFFFFu;
constexpr unsigned SPIN_CAP = 1u << 16;
constexpr int AUX_SC1 = 16;

struct Params { const u16* x; const u16* wqkv; const u16* wo; const u16* wr; const u16* wg; const u16* wu; const u16* wd; u16* out; unsigned char* ws; };

#define WAITV(n) asm volatile("s_waitcnt vmcnt(" #n ")" ::: "memory")
#define WAITL(n) asm volatile("s_waitcnt lgkmcnt(" #n ")" ::: "memory")
#define BAR() __builtin_amdgcn_s_barrier()
#define CBAR() asm volatile("" ::: "memory")
#define LBAR() do { WAITL(0); BAR(); CBAR(); } while (0)
#define PIN(a) asm volatile("" : "+v"(a))
#define SGB(m, n) __builtin_amdgcn_sched_group_barrier(m, n, 0)
#define SB() __builtin_amdgcn_sched_barrier(0)
#define RFL(x) __builtin_amdgcn_readfirstlane(x)

#define STAMP(i)
#define STAMPW(i, w)

DI unsigned pk2(float a, float b) { f32x2 v = {a, b}; bf16x2 r = __builtin_convertvector(v, bf16x2); return __builtin_bit_cast(unsigned, r); }
DI float rbf(float f) { return __uint_as_float(pk2(f, 0.f) << 16); }
DI float lo16(unsigned x) { return __uint_as_float(x << 16); }
DI float hi16(unsigned x) { return __uint_as_float(x & 0xFFFF0000u); }
DI float dot2(unsigned a, unsigned b, float c) { return __builtin_amdgcn_fdot2_f32_bf16(__builtin_bit_cast(bf16x2, a), __builtin_bit_cast(bf16x2, b), c, false); }
DI bf16x4 trread(lptr p) { return __builtin_amdgcn_ds_read_tr16_b64_v4bf16((LAS bf16x4*)p); }
DI bf16x8 trread2(lptr p, int off2) { bf16x4 a = trread(p), b = trread(p + off2); return __builtin_shufflevector(a, b, 0, 1, 2, 3, 4, 5, 6, 7); }
DI bf16x8 asbf(v4u v) { return __builtin_bit_cast(bf16x8, v); }
template <int CTRL> DI float dppf(float x) { return __builtin_bit_cast(float, __builtin_amdgcn_update_dpp(0, __builtin_bit_cast(int, x), CTRL, 0xF, 0xF, true)); }
template <int CTRL> DI unsigned dppu(unsigned x) { return (unsigned)__builtin_amdgcn_update_dpp(0, (int)x, CTRL, 0xF, 0xF, true); }
DI float rowsum16(float s) { s += dppf<0x128>(s); s += dppf<0x124>(s); s += dppf<0x122>(s); s += dppf<0x121>(s); return s; }
DI unsigned umax(unsigned a, unsigned b) { return a > b ? a : b; }
DI float xrowsum(float s) {
  auto a = __builtin_amdgcn_permlane16_swap(__float_as_uint(s), __float_as_uint(s), false, false);
  s = __uint_as_float(a[0]) + __uint_as_float(a[1]);
  auto b = __builtin_amdgcn_permlane32_swap(__float_as_uint(s), __float_as_uint(s), false, false);
  return __uint_as_float(b[0]) + __uint_as_float(b[1]);
}
DI unsigned san(unsigned x) { return x == SENT ? 0x7FC07FC0u : x; }
DI void dma16(unsigned voff, const GAS void* sbase, unsigned ldsaddr) {
  asm volatile("s_mov_b32 m0, %2\n\ts_nop 0\n\tglobal_load_lds_dwordx4 %0, %1" ::"v"(voff), "s"(sbase), "s"(ldsaddr) : "memory", "m0");
}
DI void dma16_sc1(unsigned voff, const GAS void* sbase, unsigned ldsaddr) {
  asm volatile("s_mov_b32 m0, %2\n\ts_nop 0\n\tglobal_load_lds_dwordx4 %0, %1 sc1" ::"v"(voff), "s"(sbase), "s"(ldsaddr) : "memory", "m0");
}
DI v4u ld128(rsrc_t rs, unsigned off) { return __builtin_amdgcn_raw_buffer_load_b128(rs, off, 0, AUX_SC1); }
DI v2u ld64(rsrc_t rs, unsigned off) { return __builtin_amdgcn_raw_buffer_load_b64(rs, off, 0, AUX_SC1); }
DI unsigned ld32(rsrc_t rs, unsigned off) { return __builtin_amdgcn_raw_buffer_load_b32(rs, off, 0, AUX_SC1); }
DI void st128(rsrc_t rs, unsigned off, v4u v) { __builtin_amdgcn_raw_buffer_store_b128(v, rs, off, 0, AUX_SC1); }
DI void st64(rsrc_t rs, unsigned off, v2u v) { __builtin_amdgcn_raw_buffer_store_b64(v, rs, off, 0, AUX_SC1); }
DI void st32(rsrc_t rs, unsigned off, unsigned v) { __builtin_amdgcn_raw_buffer_store_b32(v, rs, off, 0, AUX_SC1); }
DI void poll_flags(rsrc_t rs, unsigned off, bool need, unsigned tlo, unsigned thi) {
  for (unsigned sp = 0; sp < SPIN_CAP; ++sp) {
    CBAR();
    v2u f = ld64(rs, off);
    if (__all(!need || (f.x == tlo && f.y == thi))) break;
  }
}
#define MFMA32(a, b, c) __builtin_amdgcn_mfma_f32_32x32x16_bf16(a, b, c, 0, 0, 0)
#define MFMA16(a, b, c) __builtin_amdgcn_mfma_f32_16x16x32_bf16(a, b, c, 0, 0, 0)

__global__ void __launch_bounds__(512, 2) mega_kernel(Params p) {
  extern __shared__ __attribute__((aligned(16))) unsigned char lds_[];
  LAS unsigned char* lds = (LAS unsigned char*)lds_;
  const unsigned lds0 = (unsigned)(size_t)lds;
  const int wg = blockIdx.x, tid = threadIdx.x, lane = tid & 63, wave = RFL(tid >> 6);
  const int xcd = wg & 7, loc = wg >> 3;
  GAS unsigned char* ws = (GAS unsigned char*)p.ws;
  const rsrc_t rs = __builtin_amdgcn_make_buffer_rsrc((void*)p.ws, 0, WS_BYTES, 0x00020000);
  const int h = lane >> 5, t = lane & 31;
  STAMP(0);
  const int rb = 4 * xcd + (loc >> 3), mm = loc & 7;
  const bool leader = (mm == 7);
  const int nl = loc - (loc >> 3);
  const int fq = xcd + 8 * (nl >> 2);
  const bool is_gu = !leader && nl < 16 && fq < 27;  const int fsl = 4 * fq + (nl & 3);
  const int dq = xcd + 8 * ((nl - 16) >> 2);
  const bool is_dn = !leader && nl >= 16 && nl < 24 && dq < 10;  const int dsl = 4 * dq + (nl & 3);
  unsigned tlo, thi;

  const GAS unsigned char* Xb = (const GAS unsigned char*)p.x;
  if (loc >= 30) {
    const int tb = xcd * 2 + (loc - 30);
    const GAS unsigned char* xb = Xb + (size_t)(tb * 128 + wave * 16 + (lane >> 4)) * 1280 + (lane & 15) * 16;
    v4u v[4][5];
#pragma unroll
    for (int g = 0; g < 4; ++g)
#pragma unroll
      for (int j = 0; j < 5; ++j) v[g][j] = *(const GAS v4u*)(xb + g * 4 * 1280 + j * 256);
    v2u e = ld64(rs, WS_EPOCH);
    SB();
    { u64 T = (((u64)e.y << 32) | e.x) + 1; tlo = RFL((unsigned)T); thi = RFL((unsigned)(T >> 32)); }
    const unsigned ONES = 0x3F803F80u;
#pragma unroll
    for (int g = 0; g < 4; ++g) {
      float s = 0.f, q = 0.f;
#pragma unroll
      for (int j = 0; j < 5; ++j)
#pragma unroll
        for (int c = 0; c < 4; ++c) { s = dot2(v[g][j][c], ONES, s); q = dot2(v[g][j][c], v[g][j][c], q); }
      s = rowsum16(s); q = rowsum16(q);
      const float mean = s * (1.f / 640.f), var = fmaxf(q * (1.f / 640.f) - mean * mean, 0.f);
      const float mb = rbf(mean), sd = rbf(sqrtf(rbf(rbf(var) + 1e-5f)));
      if ((lane & 15) == 0) st64(rs, WS_TS + (tb * 128 + wave * 16 + g * 4 + (lane >> 4)) * 8, v2u{(__float_as_uint(mb) >> 16) | (__float_as_uint(sd) & 0xFFFF0000u), tlo});
    }
  } else {
    const int ti = xcd * 30 + loc, ci = ti / 5, cbi = ti - 5 * ci, tb = ci & 15, cb = (ci >> 4) * 5 + cbi;
    const int c0 = (tb + 2 * cb) % 10;
    LAS float* stm = (LAS float*)(lds + L_MISC); LAS float* str = stm + 128; LAS float* csp = stm + 256;
    if (wave == 4 || wave == 5) {
      const int lw = wave - 4;
      const GAS unsigned char* Xg = Xb + (size_t)tb * 128 * 1280;
      const GAS unsigned char* Wg = (const GAS unsigned char*)p.wqkv + cb * 256;
      unsigned xoff[8], woff[8];
#pragma unroll
      for (int i = 0; i < 8; ++i) {
        const int ins = lw * 8 + i, row = ins * 8 + (lane >> 3), r2 = ins * 4 + (lane >> 4);
        xoff[i] = row * 1280 + (((lane & 7) ^ ((row >> 1) & 7)) << 4);
        woff[i] = r2 * 3840 + (((lane & 15) ^ (4 * (r2 & 3))) << 4);
        asm volatile("" : "+v"(xoff[i]), "+v"(woff[i]));
      }
      auto issue = [&](int ci_) {
        int c = ci_ + c0; c = c >= 10 ? c - 10 : c;
        const unsigned dst = lds0 + (ci_ & 3) * 32768 + lw * 8192;
        const GAS unsigned char* xb = Xg + c * 128; const GAS unsigned char* wb = Wg + (size_t)c * 64 * 3840;
#pragma unroll
        for (int i = 0; i < 8; ++i) { dma16(xoff[i], xb, dst + i * 1024); dma16(woff[i], wb, dst + 16384 + i * 1024); }
      };
      issue(0); issue(1); issue(2);
      WAITV(32); BAR(); issue(3);
#pragma unroll 1
      for (int c = 1; c < 7; ++c) { WAITV(32); BAR(); issue(c + 3); }
      WAITV(32); BAR();
      WAITV(16); BAR();
      WAITV(0); BAR();
      v2u e = ld64(rs, WS_EPOCH);
      { u64 T = (((u64)e.y << 32) | e.x) + 1; tlo = RFL((unsigned)T); thi = RFL((unsigned)(T >> 32)); }
    } else if (wave >= 6) {
      const int cw = wave - 6;
      v2u e = ld64(rs, WS_EPOCH);
      const v4u s4 = {SENT, SENT, SENT, SENT};
      if (cw == 0) st128(rs, WS_S + (rb * 8 + mm) * 1024 + lane * 16, s4);
      else {
        if (leader && lane < 32) st128(rs, WS_RK + rb * 512 + lane * 16, s4);
        if (is_gu && lane < 16) st128(rs, WS_ACT + fsl * 256 + lane * 16, s4);
      }
      { u64 T = (((u64)e.y << 32) | e.x) + 1; tlo = RFL((unsigned)T); thi = RFL((unsigned)(T >> 32)); }
      v4u w[8];
      BAR();
      if (tb < 10) {
        lptr sb = lds + 16384 + (cw * 32 + (lane >> 4)) * 256 + (lane & 15) * 16;
#pragma unroll
        for (int i = 0; i < 8; ++i) w[i] = *(const LAS v4u*)(sb + i * 1024);
#pragma unroll
        for (int i = 0; i < 8; ++i) PIN(w[i]);
      }
      BAR();
      float cs[8] = {0, 0, 0, 0, 0, 0, 0, 0};
#define CSACC(i) { _Pragma("unroll") for (int e_ = 0; e_ < 4; ++e_) { cs[2 * e_] += lo16(w[i][e_]); cs[2 * e_ + 1] += hi16(w[i][e_]); } }
      if (tb < 10) { CSACC(0) CSACC(1) CSACC(2) _Pragma("unroll") for (int e_ = 0; e_ < 8; ++e_) PIN(cs[e_]); }
      BAR();
      if (tb < 10) { CSACC(3) CSACC(4) CSACC(5) _Pragma("unroll") for (int e_ = 0; e_ < 8; ++e_) PIN(cs[e_]); }
      BAR();
      if (tb < 10) {
        CSACC(6) CSACC(7)
        LAS float* dst = csp + (cw * 4 + (lane >> 4)) * 128 + (((lane & 15) ^ (4 * (lane >> 4))) * 8);
        *(LAS f32x4*)dst = f32x4{cs[0], cs[1], cs[2], cs[3]}; *(LAS f32x4*)(dst + 4) = f32x4{cs[4], cs[5], cs[6], cs[7]};
        WAITL(0);
      }
      BAR();
      if (tb < 10) {
        float a = 0.f;
#pragma unroll
        for (int g = 0; g < 8; ++g) a += csp[g * 128 + cw * 64 + lane];
        st64(rs, WS_CS + (cb * 10 + c0) * 1024 + (cw * 64 + lane) * 8, v2u{__float_as_uint(a), tlo});
      }
      BAR();
      BAR();
      WAITV(0);
      BAR();
      if (cw == 0 && lane == 0) st64(rs, WS_CF + wg * 8, v2u{tlo, thi});
      v2u g0 = {0, 0}, g1 = {0, 0}; v4u pp[10];
      if (cw == 0) { g0 = ld64(rs, WS_TS + (tb * 128 + lane) * 8); g1 = ld64(rs, WS_TS + (tb * 128 + 64 + lane) * 8); }
      else {
#pragma unroll
        for (int k = 0; k < 10; ++k) pp[k] = ld128(rs, WS_CS + (cb * 10 + k) * 1024 + lane * 16);
      }
      BAR();
      BAR();
      if (cw == 0) {
        for (unsigned sp = 0; sp < SPIN_CAP; ++sp) {
          if (__all(g0.y == tlo && g1.y == tlo)) break;
          CBAR();
          g0 = ld64(rs, WS_TS + (tb * 128 + lane) * 8); g1 = ld64(rs, WS_TS + (tb * 128 + 64 + lane) * 8);
        }
        stm[lane] = lo16(g0.x); str[lane] = 1.f / hi16(g0.x); stm[64 + lane] = lo16(g1.x); str[64 + lane] = 1.f / hi16(g1.x);
      } else {
        for (unsigned sp = 0; sp < SPIN_CAP; ++sp) {
          bool ok = true;
#pragma unroll
          for (int k = 0; k < 10; ++k) ok &= (pp[k].y == tlo) && (pp[k].w == tlo);
          if (__all(ok)) break;
          CBAR();
#pragma unroll
          for (int k = 0; k < 10; ++k) pp[k] = ld128(rs, WS_CS + (cb * 10 + k) * 1024 + lane * 16);
        }
        float a = 0.f, b = 0.f;
#pragma unroll
        for (int k = 0; k < 10; ++k) { a += __uint_as_float(pp[k].x); b += __uint_as_float(pp[k].z); }
        csp[16 * 128 + 2 * lane] = a; csp[16 * 128 + 2 * lane + 1] = b;
      }
    } else {
      v2u e = ld64(rs, WS_EPOCH);
      const int th = wave & 1, tp = wave >> 1;
      unsigned xa[4];
#pragma unroll
      for (int s = 0; s < 4; ++s) xa[s] = (64 * th + t) * 128 + (((2 * s + h) ^ ((t >> 1) & 7)) << 4);
      const int G = lane >> 4, i16 = lane & 15, q = i16 >> 2, pp = i16 & 3, hh = G >> 1, fb = G & 1;
      unsigned wa[2];
#pragma unroll
      for (int j = 0; j < 2; ++j) wa[j] = 16384 + (8 * hh + q) * 256 + (((((tp * 2 + j) ^ q) << 2) | (fb << 1) | (pp >> 1)) << 4) + (pp & 1) * 8;
      f32x16 acc[2][2];
#pragma unroll
      for (int i = 0; i < 2; ++i)
#pragma unroll
        for (int j = 0; j < 2; ++j)
#pragma unroll
          for (int r = 0; r < 16; ++r) acc[i][j][r] = 0.f;
      v4u xf[2][2]; bf16x8 wf[2][2];
      auto ld = [&](int b, lptr sb, int s) {
        xf[b][0] = *(const LAS v4u*)(sb + xa[s]); xf[b][1] = *(const LAS v4u*)(sb + xa[s] + 4096);
#pragma unroll
        for (int j = 0; j < 2; ++j) wf[b][j] = trread2(sb + wa[j] + (16 * s) * 256, 1024);
      };
      auto mma = [&](int b) {
#pragma unroll
        for (int j = 0; j < 2; ++j)
#pragma unroll
          for (int i = 0; i < 2; ++i) acc[i][j] = MFMA32(wf[b][j], asbf(xf[b][i]), acc[i][j]);
      };
#define GRP() SGB(0x8, 1); SGB(0x100, 2); SGB(0x8, 1); SGB(0x100, 2); SGB(0x8, 1); SGB(0x100, 2); SGB(0x8, 1); SB()
      { u64 T = (((u64)e.y << 32) | e.x) + 1; tlo = RFL((unsigned)T); thi = RFL((unsigned)(T >> 32)); }
      BAR();
      CBAR();
      STAMP(1);
      ld(0, lds, 0);
#pragma unroll 1
      for (int c = 0; c < 10; ++c) {
        lptr sb = lds + (c & 3) * 32768;
        ld(1, sb, 1); mma(0); GRP();
        ld(0, sb, 2); mma(1); GRP();
        ld(1, sb, 3); mma(0); GRP();
        if (c < 9) { WAITL(0); BAR(); CBAR(); }
        ld(0, lds + ((c + 1) & 3) * 32768, 0);
        mma(1); GRP();
      }
      STAMP(2);
      LBAR();
      const float qs = cb < 5 ? QSCALE : 1.f;
      const LAS float* cs = csp + 16 * 128;
#pragma unroll
      for (int i = 0; i < 2; ++i) {
        const int tt = 2 * th + i;
        const float nm = -stm[tt * 32 + t], rsd = str[tt * 32 + t] * qs;
#pragma unroll
        for (int j = 0; j < 2; ++j) {
          const int tile = tp * 2 + j;
          unsigned P[4][2];
#pragma unroll
          for (int g = 0; g < 4; ++g) {
            const f32x4 c4 = *(const LAS f32x4*)(cs + tile * 32 + 8 * g + 4 * h);
            f32x2 a = {acc[i][j][4 * g], acc[i][j][4 * g + 1]}, b = {acc[i][j][4 * g + 2], acc[i][j][4 * g + 3]};
            const f32x2 ca = {c4[0], c4[1]}, cb2 = {c4[2], c4[3]}, nm2 = {nm, nm}, rs2 = {rsd, rsd};
            a = (ca * nm2 + a) * rs2; b = (cb2 * nm2 + b) * rs2;
            P[g][0] = san(pk2(a[0], a[1])); P[g][1] = san(pk2(b[0], b[1]));
          }
#pragma unroll
          for (int gp = 0; gp < 2; ++gp) {
            auto r0 = __builtin_amdgcn_permlane32_swap(P[2 * gp][0], P[2 * gp + 1][0], false, false);
            auto r1 = __builtin_amdgcn_permlane32_swap(P[2 * gp][1], P[2 * gp + 1][1], false, false);
            st128(rs, WS_QKV + ((tb * 240 + cb * 16 + tile * 4 + 2 * gp + h) * 128 + tt * 32 + t) * 16, v4u{r0[0], r1[0], r0[1], r1[1]});
          }
        }
      }
      STAMP(3);
      WAITV(0);
    }
    if (wave >= 4) { LBAR(); }
    BAR();
    CBAR();
    if (tid == 0) st64(rs, WS_QF + (tb * 15 + cb) * 128, v2u{tlo, thi});
  }
  if (loc >= 30) {
    const v4u s4 = {SENT, SENT, SENT, SENT};
    if (wave == 6) st128(rs, WS_S + (rb * 8 + mm) * 1024 + lane * 16, s4);
    if (wave == 7) {
      if (leader && lane < 32) st128(rs, WS_RK + rb * 512 + lane * 16, s4);
      if (is_gu && lane < 16) st128(rs, WS_ACT + fsl * 256 + lane * 16, s4);
    }
    WAITV(0); BAR(); CBAR();
    if (tid == 0) st64(rs, WS_CF + wg * 8, v2u{tlo, thi});
  }
  STAMP(4);

  {
    const int hp = 2 * xcd + (loc & 1), tb = loc >> 1;
    const int cq0 = (5 * hp) >> 4, cq1 = (5 * hp + 4) >> 4;
    if (tid < 64) *(LAS v2u*)(lds + L_ONE + tid * 8) = v2u{0x00003F80u, 0u};
    {
      const int third = lane < 2 ? 0 : (lane < 8 ? 1 : 2), jj = lane < 2 ? 0 : ((lane - 2) % 6) >> 1, which = lane & 1;
      const int tbx = tb - jj;
      const bool need = lane < 14 && tbx >= 0;
      poll_flags(rs, WS_QF + ((need ? tbx : 0) * 15 + third * 5 + (which ? cq1 : cq0)) * 128, need, tlo, thi);
    }
    STAMP(5);
    const GAS unsigned char* qkv = ws + WS_QKV;
#pragma unroll
    for (int i = 0; i < 4; ++i) {
      const int n = wave + 8 * i;
      if (n < 30) {
        const int u = n / 6, r = n - 6 * u, j = r >> 1, hf = r & 1, tbx = tb - 2 + j;
        if (tbx >= 0) dma16_sc1((unsigned)lane * 16, qkv + ((size_t)(tbx * 240 + 80 + 5 * hp + u) * 128 + hf * 64) * 16, lds0 + L_K + u * 6144 + j * 2048 + hf * 1024);
      }
    }
    const int a = wave >> 2, j4 = wave & 3;
    v4u q0, q1;
    {
      const unsigned qb = WS_QKV + ((tb * 240 + 5 * hp) * 128 + j4 * 32 + t) * 16;
      q0 = ld128(rs, qb + (a ? 3 + h : h) * 2048);
      q1 = ld128(rs, qb + 2 * 2048);
    }
#pragma unroll
    for (int i = 0; i < 4; ++i) {
      const int n = wave + 8 * i;
      if (n < 30) {
        const int u = n / 6, r = n - 6 * u, j = r >> 1, hf = r & 1, tbx = tb - 2 + j;
        const unsigned src = ((lane & 48) | ((((lane >> 2) - u) & 3) << 2) | (lane & 3)) * 16;
        if (tbx >= 0) dma16_sc1(src, qkv + ((size_t)(tbx * 240 + 160 + 5 * hp + u) * 128 + hf * 64) * 16, lds0 + L_V + u * 6144 + j * 2048 + hf * 1024);
      }
    }
    PIN(q0); PIN(q1);
    q1 = h ? v4u{0, 0, 0, 0} : (a ? v4u{0, 0, q1.z, q1.w} : v4u{q1.x, q1.y, 0, 0});
    WAITV(0); LBAR();
    STAMP(6);
    f32x16 mD, mE;
#pragma unroll
    for (int r = 0; r < 16; ++r) { const int kl = (r & 3) + 8 * (r >> 2) + 4 * h; mD[r] = kl <= t ? 0.f : -INFINITY; mE[r] = kl > t ? 0.f : -INFINITY; }
    const int i0 = max(0, 8 - 4 * tb - j4);
    lptr kp0 = lds + L_K + (a ? 3 + h : h) * 6144 + (j4 * 32 + t) * 16;
    lptr kp1 = lds + L_K + 2 * 6144 + (j4 * 32 + t) * 16;
    const bf16x8 Q0 = asbf(q0), Q1 = asbf(q1);
    f32x16 zero;
#pragma unroll
    for (int r = 0; r < 16; ++r) zero[r] = 0.f;
    auto scores = [&](int i, const f32x16& c) {
      const bf16x8 k0 = asbf(*(const LAS v4u*)(kp0 + i * 512)), k1 = asbf(*(const LAS v4u*)(kp1 + i * 512));
      f32x16 s = MFMA32(k0, Q0, c);
      return MFMA32(k1, Q1, s);
    };
    auto rmax = [&](const f32x16& s, float m) {
#pragma unroll
      for (int r = 0; r < 16; r += 2) m = __builtin_fmaxf(__builtin_fmaxf(m, s[r]), s[r + 1]);
      return m;
    };
    float m = rmax(scores(8, mD), -INFINITY);
    if (i0 == 0) m = rmax(scores(0, mE), m);
#pragma unroll 1
    for (int i = max(i0, 1); i < 8; ++i) m = rmax(scores(i, zero), m);
    { auto r = __builtin_amdgcn_permlane32_swap(__float_as_uint(m), __float_as_uint(m), false, false); m = __builtin_fmaxf(__uint_as_float(r[0]), __uint_as_float(r[1])); }
    lptr vp[2]; unsigned vstep;
    {
      const int G = lane >> 4, q = (lane & 15) >> 2, pp = lane & 3, hh = G >> 1, c = 4 * (G & 1) + pp;
      const int cc = a ? c + 1 : c;
      const int gc = a ? 5 + c : c, u = gc >> 1;
      (void)cc;
#pragma unroll
      for (int t2 = 0; t2 < 2; ++t2) {
        const int kg = hh + 2 * t2;
        const unsigned data = L_V + u * 6144 + j4 * 512 + (((kg + u) & 3) << 6) + q * 16 + (gc & 1) * 8;
        const unsigned cons = L_ONE + (((kg + (a ? 1 : 3)) & 3) << 6) + q * 16;
        vp[t2] = lds + (c < 5 ? data : cons);
      }
      vstep = c < 5 ? 512u : 0u;
    }
    f32x16 O = zero;
    auto tile = [&](int i, const f32x16& c) {
      f32x16 s = scores(i, c);
      unsigned P[8];
#pragma unroll
      for (int r = 0; r < 8; ++r) P[r] = pk2(__builtin_amdgcn_exp2f(s[2 * r]), __builtin_amdgcn_exp2f(s[2 * r + 1]));
#pragma unroll
      for (int s2 = 0; s2 < 2; ++s2) {
        const bf16x4 va = trread(vp[0] + i * vstep + s2 * 256), vb = trread(vp[1] + i * vstep + s2 * 256);
        const bf16x8 vf = __builtin_shufflevector(va, vb, 0, 1, 2, 3, 4, 5, 6, 7);
        O = MFMA32(vf, asbf(v4u{P[4 * s2], P[4 * s2 + 1], P[4 * s2 + 2], P[4 * s2 + 3]}), O);
      }
    };
    {
      f32x16 c;
#pragma unroll
      for (int r = 0; r < 16; ++r) c[r] = mD[r] - m;
      tile(8, c);
      if (i0 == 0) {
#pragma unroll
        for (int r = 0; r < 16; ++r) c[r] = mE[r] - m;
        tile(0, c);
      }
#pragma unroll
      for (int r = 0; r < 16; ++r) c[r] = -m;
#pragma unroll 1
      for (int i = max(i0, 1); i < 8; ++i) tile(i, c);
    }
    STAMP(7);
    {
      auto lr = __builtin_amdgcn_permlane32_swap(__float_as_uint(O[8]), __float_as_uint(O[8]), false, false);
      const float li = 1.f / __uint_as_float(lr[1]);
      unsigned G0[2], G1[2], G2[2];
      G0[0] = san(pk2(O[0] * li, O[1] * li)); G0[1] = san(pk2(O[2] * li, O[3] * li));
      G1[0] = san(pk2(O[4] * li, O[5] * li)); G1[1] = san(pk2(O[6] * li, O[7] * li));
      G2[0] = san(pk2(O[8] * li, O[9] * li)); G2[1] = san(pk2(O[10] * li, O[11] * li));
      auto r0 = __builtin_amdgcn_permlane32_swap(G0[0], G1[0], false, false);
      auto r1 = __builtin_amdgcn_permlane32_swap(G0[1], G1[1], false, false);
      const int tok = tb * 128 + j4 * 32 + t;
      const unsigned ob = WS_OU + (tok >> 4) * 20480 + (tok & 15) * 16;
      const int u = 5 * hp + 2 * a + h, u4 = 5 * hp + 4;
      st128(rs, ob + (u >> 2) * 1024 + (u & 3) * 256, v4u{r0[0], r1[0], r0[1], r1[1]});
      if (h == 0) st64(rs, ob + (u4 >> 2) * 1024 + (u4 & 3) * 256 + a * 8, v2u{G2[0], G2[1]});
    }
    WAITV(0); LBAR();
    if (tid == 0) st64(rs, WS_OF + (tb * 16 + hp) * 128, v2u{tlo, thi});
    STAMP(8);
  }

  const int rt = wave & 3, kh = wave >> 2, g4 = lane >> 4, tk = lane & 15;
  unsigned hp2[5][2];
  {
    {
      const unsigned voff = (lane / 10) * 1280 + (lane % 10) * 16;
      const GAS unsigned char* wob = (const GAS unsigned char*)p.wo + mm * 160;
      if (lane < 40) {
#pragma unroll 1
        for (int i = 0; i < 20; ++i) {
          const int G4 = wave + 8 * i, s = G4 >> 3, r = G4 & 7, g = ((r >> 2) << 1) | (r & 1), t2 = (r >> 1) & 1;
          const int u = 4 * s + g, hq = u / 5, j5 = u - 5 * hq;
          const int row0 = 40 * hq + (j5 == 4 ? (t2 ? 36 : 16) : ((j5 >> 1) * 20 + (j5 & 1) * 8 + 4 * t2));
          dma16(voff, wob + row0 * 1280, lds0 + L_WO + G4 * 640);
        }
      }
    }
    v2u xr[5], wr4[5];
#pragma unroll
    for (int ct = 0; ct < 5; ++ct) {
      xr[ct] = *(const GAS v2u*)(Xb + (size_t)(64 * rb + 16 * rt + tk) * 1280 + (80 * mm + 16 * ct + 4 * g4) * 2);
      wr4[ct] = *(const GAS v2u*)((const GAS unsigned char*)p.wr + (80 * mm + 16 * ct + 4 * g4) * 2);
    }
    if (wave == 7) {
      for (unsigned sp = 0; sp < SPIN_CAP; ++sp) {
        CBAR();
        const v4u f0 = ld128(rs, WS_CF + lane * 32), f1 = ld128(rs, WS_CF + lane * 32 + 16);
        if (__all(f0.x == tlo && f0.y == thi && f0.z == tlo && f0.w == thi && f1.x == tlo && f1.y == thi && f1.z == tlo && f1.w == thi)) break;
      }
    }
    poll_flags(rs, WS_OF + ((rb >> 1) * 16 + (lane & 15)) * 128, lane < 16, tlo, thi);
    STAMP(9);
    v4u of[10];
#pragma unroll
    for (int s = 0; s < 10; ++s) of[s] = ld128(rs, WS_OU + (4 * rb + rt) * 20480 + (10 * kh + s) * 1024 + lane * 16);
#pragma unroll
    for (int s = 0; s < 10; ++s) PIN(of[s]);
    WAITV(0); LBAR();
    STAMP(10);
    f32x4 acc[5];
#pragma unroll
    for (int ct = 0; ct < 5; ++ct) acc[ct] = f32x4{0, 0, 0, 0};
    lptr wb = lds + L_WO + (kh * 320 + 16 * (g4 >> 1) + 4 * (g4 & 1) + (tk >> 2)) * 160 + (tk & 3) * 8;
#pragma unroll
    for (int s = 0; s < 10; ++s)
#pragma unroll
      for (int ct = 0; ct < 5; ++ct) acc[ct] = MFMA16(trread2(wb + s * 32 * 160 + ct * 32, 8 * 160), asbf(of[s]), acc[ct]);
    if (kh == 1) {
#pragma unroll
      for (int ct = 0; ct < 5; ++ct) *(LAS f32x4*)(lds + L_RED + (rt * 5 + ct) * 1024 + lane * 16) = acc[ct];
    }
    LBAR();
    STAMP(11);
    if (kh == 0) {
      float s1 = 0.f, s2 = 0.f, s3 = 0.f;
#pragma unroll
      for (int ct = 0; ct < 5; ++ct) {
        const f32x4 o2 = *(const LAS f32x4*)(lds + L_RED + (rt * 5 + ct) * 1024 + lane * 16);
        const float a0 = rbf(acc[ct][0] + o2[0]), a1 = rbf(acc[ct][1] + o2[1]), a2 = rbf(acc[ct][2] + o2[2]), a3 = rbf(acc[ct][3] + o2[3]);
        hp2[ct][0] = san(pk2(lo16(xr[ct].x) + a0, hi16(xr[ct].x) + a1));
        hp2[ct][1] = san(pk2(lo16(xr[ct].y) + a2, hi16(xr[ct].y) + a3));
        const float h0 = lo16(hp2[ct][0]), h1 = hi16(hp2[ct][0]), h2 = lo16(hp2[ct][1]), h3 = hi16(hp2[ct][1]);
        s1 += (h0 + h1) + (h2 + h3);
        s2 = fmaf(h0, h0, fmaf(h1, h1, fmaf(h2, h2, fmaf(h3, h3, s2))));
        s3 = fmaf(h0, lo16(wr4[ct].x), fmaf(h1, hi16(wr4[ct].x), fmaf(h2, lo16(wr4[ct].y), fmaf(h3, hi16(wr4[ct].y), s3))));
      }
      s1 = xrowsum(s1); s2 = xrowsum(s2); s3 = xrowsum(s3);
      if (g4 == 0) st128(rs, WS_S + (rb * 8 + mm) * 1024 + (16 * rt + tk) * 16, v4u{san(__float_as_uint(s1)), san(__float_as_uint(s2)), san(__float_as_uint(s3)), 0u});
#pragma unroll
      for (int ct = 0; ct < 5; ++ct) st64(rs, WS_H + ((rb * 8 + mm) * 5 + ct) * 2048 + (16 * rt + tk) * 32 + g4 * 8, v2u{hp2[ct][0], hp2[ct][1]});
      WAITV(0);
    }
    BAR(); CBAR();
    if (tid == 0) st64(rs, WS_HD + rb * 64 + mm * 8, v2u{tlo, thi});
    STAMP(12);
  }

  if (is_gu) {
    const unsigned voff = (lane >> 1) * 3456 + (lane & 1) * 16;
#pragma unroll 1
    for (int i = 0; i < 5; ++i) {
      const int n = wave + 8 * i, mat = n >= 20, blk = n - 20 * mat;
      dma16(voff, (const GAS unsigned char*)(mat ? p.wu : p.wg) + fsl * 32 + (size_t)blk * 32 * 3456, lds0 + n * 1024);
    }
  } else if (is_dn) {
    const unsigned voff = (lane >> 1) * 1280 + (lane & 1) * 16;
#pragma unroll 1
    for (int n = wave; n < 54; n += 8) dma16(voff, (const GAS unsigned char*)p.wd + dsl * 32 + (size_t)n * 32 * 1280, lds0 + n * 1024);
  }
  {
    v4u sp4;
    for (unsigned sp = 0; sp < SPIN_CAP; ++sp) {
      CBAR();
      sp4 = ld128(rs, WS_S + rb * 8192 + tid * 16);
      if (__all(sp4.x != SENT && sp4.y != SENT && sp4.z != SENT)) break;
    }
    *(LAS v4u*)(lds + L_SG + tid * 16) = sp4;
    LBAR();
    if (wave == 0) {
      float s1 = 0.f, s2 = 0.f, s3 = 0.f;
#pragma unroll
      for (int k = 0; k < 8; ++k) { const f32x4 v = *(const LAS f32x4*)(lds + L_SG + k * 1024 + lane * 16); s1 += v[0]; s2 += v[1]; s3 += v[2]; }
      const float mean = s1 * (1.f / 640.f), var = fmaxf(s2 * (1.f / 640.f) - mean * mean, 0.f);
      const float mb = rbf(mean), sd = rbf(sqrtf(rbf(rbf(var) + 1e-5f)));
      *(LAS f32x2*)(lds + L_ST + lane * 8) = f32x2{mb, sd};
      if (leader) {
        float W = 0.f;
        {
          const GAS unsigned* wr32 = (const GAS unsigned*)p.wr;
#pragma unroll
          for (int k = 0; k < 5; ++k) { const unsigned w2 = wr32[k * 64 + lane]; W += lo16(w2) + hi16(w2); }
          W = xrowsum(rowsum16(W));
        }
        const unsigned rbits = pk2((s3 - mb * W) / sd, 0.f) & 0xFFFFu;
        const unsigned ord = (rbits & 0x8000u) ? (~rbits & 0xFFFFu) : (rbits | 0x8000u);
        const unsigned key = (ord << 16) | (2047u - (64 * rb + lane));
        st32(rs, WS_RK + rb * 512 + 128 + lane * 4, san((__float_as_uint(mb) >> 16) | (__float_as_uint(sd) & 0xFFFF0000u)));
        *(LAS unsigned*)(lds + L_KEY + lane * 4) = key;
      }
    }
    LBAR();
    STAMP(13);
    if (leader && wave == 0) {
      const unsigned key = *(const LAS unsigned*)(lds + L_KEY + lane * 4);
      unsigned rank = 0;
#pragma unroll
      for (int k = 0; k < 16; ++k) { const v4u o4 = *(const LAS v4u*)(lds + L_KEY + k * 16); rank += (o4.x > key) + (o4.y > key) + (o4.z > key) + (o4.w > key); }
      poll_flags(rs, WS_HD + rb * 64 + (lane & 7) * 8, lane < 8, tlo, thi);
      if (rank < 8) st32(rs, WS_RK + rb * 512 + rank * 4, key);
    }
  }
  STAMP(14);
  unsigned sel[8];
  {
    v4u ka, kb;
    for (unsigned sp = 0; sp < SPIN_CAP; ++sp) {
      CBAR();
      ka = ld128(rs, WS_RK + t * 512); kb = ld128(rs, WS_RK + t * 512 + 16);
      if (__all(ka.x != SENT && ka.y != SENT && ka.z != SENT && ka.w != SENT && kb.x != SENT && kb.y != SENT && kb.z != SENT && kb.w != SENT)) break;
    }
    STAMP(15);
    unsigned k[8] = {ka.x, ka.y, ka.z, ka.w, kb.x, kb.y, kb.z, kb.w};
#pragma unroll
    for (int i = 0; i < 8; ++i) {
      unsigned mx = k[0];
      mx = umax(mx, dppu<0x128>(mx)); mx = umax(mx, dppu<0x124>(mx)); mx = umax(mx, dppu<0x122>(mx)); mx = umax(mx, dppu<0x121>(mx));
      const unsigned best = umax((unsigned)__builtin_amdgcn_readlane((int)mx, 0), (unsigned)__builtin_amdgcn_readlane((int)mx, 16));
      sel[i] = best;
      const bool win = k[0] == best;
#pragma unroll
      for (int jx = 0; jx < 7 - i; ++jx) k[jx] = win ? k[jx + 1] : k[jx];
    }
  }
  if (wg == 0 && tid == 0) st64(rs, WS_EPOCH, v2u{tlo, thi});
  STAMP(16);
  const rsrc_t ro = __builtin_amdgcn_make_buffer_rsrc((void*)p.out, 0, 2048 * 1280, 0x00020000);
  unsigned mykey = sel[0];
#pragma unroll
  for (int i = 1; i < 8; ++i) mykey = (lane & 7) == i ? sel[i] : mykey;
  const int mytok = 2047 - (int)(mykey & 0xFFFFu);

  if (is_gu) {
    const int mat = wave & 1, kq = wave >> 1;
    v4u hx[5]; unsigned msd;
    for (unsigned sp = 0; sp < SPIN_CAP; ++sp) {
      CBAR();
      msd = ld32(rs, WS_RK + (mytok >> 6) * 512 + 128 + (mytok & 63) * 4);
      if (__all(msd != SENT)) break;
    }
#pragma unroll
    for (int s = 0; s < 5; ++s) {
      const int col = 32 * (5 * kq + s) + 8 * g4, m8 = col / 80, cr = col - 80 * m8;
      hx[s] = ld128(rs, WS_H + (((mytok >> 6) * 8 + m8) * 5 + (cr >> 4)) * 2048 + (mytok & 63) * 32 + (cr & 8) * 2);
    }
    const float mb = lo16(msd), sd = hi16(msd), rsd = 1.f / sd;
    WAITV(0); LBAR();
    f32x4 acc = {0, 0, 0, 0};
    lptr wb = lds + mat * 20480 + (kq * 160 + 8 * g4 + (tk >> 2)) * 32 + (tk & 3) * 8;
#pragma unroll
    for (int s = 0; s < 5; ++s) {
      v4u xn;
#pragma unroll
      for (int e = 0; e < 4; ++e) xn[e] = pk2(rbf(lo16(hx[s][e]) - mb) * rsd, rbf(hi16(hx[s][e]) - mb) * rsd);
      acc = MFMA16(trread2(wb + s * 32 * 32, 4 * 32), asbf(xn), acc);
    }
    *(LAS f32x4*)(lds + 40960 + wave * 1024 + lane * 16) = acc;
    LBAR();
    if (wave == 0) {
      f32x4 gs = {0, 0, 0, 0}, us = {0, 0, 0, 0};
#pragma unroll
      for (int k = 0; k < 4; ++k) { gs += *(const LAS f32x4*)(lds + 40960 + (2 * k) * 1024 + lane * 16); us += *(const LAS f32x4*)(lds + 40960 + (2 * k + 1) * 1024 + lane * 16); }
      float av[4];
#pragma unroll
      for (int e = 0; e < 4; ++e) { const float gg = rbf(gs[e]), uu = rbf(us[e]); av[e] = rbf(gg / (1.f + __expf(-gg))) * uu; }
      if (tk < 8) st64(rs, WS_ACT + (fsl >> 1) * 512 + ((fsl & 1) * 2 + (g4 >> 1)) * 128 + tk * 16 + (g4 & 1) * 8, v2u{san(pk2(av[0], av[1])), san(pk2(av[2], av[3]))});
    }
    STAMP(17);
  }
  if (kh == 0) {
    const int tok = 64 * rb + 16 * rt + tk;
    bool routed = false;
#pragma unroll
    for (int i = 0; i < 8; ++i) routed |= (2047 - (int)(sel[i] & 0xFFFFu)) == tok;
    const f32x2 st = *(const LAS f32x2*)(lds + L_ST + (16 * rt + tk) * 8);
    const float rsd = 1.f / st[1];
    if (!routed) {
#pragma unroll
      for (int ct = 0; ct < 5; ++ct) {
        const float h0 = lo16(hp2[ct][0]), h1 = hi16(hp2[ct][0]), h2 = lo16(hp2[ct][1]), h3 = hi16(hp2[ct][1]);
        const float n0 = rbf(rbf(h0 - st[0]) * rsd), n1 = rbf(rbf(h1 - st[0]) * rsd), n2 = rbf(rbf(h2 - st[0]) * rsd), n3 = rbf(rbf(h3 - st[0]) * rsd);
        st64(ro, tok * 1280 + (80 * mm + 16 * ct + 4 * g4) * 2, v2u{pk2(h0 + n0, h1 + n1), pk2(h2 + n2, h3 + n3)});
      }
    }
  }
  if (is_dn) {
    unsigned msd; v2u hv;
    for (unsigned sp = 0; sp < SPIN_CAP; ++sp) {
      CBAR();
      msd = ld32(rs, WS_RK + (mytok >> 6) * 512 + 128 + (mytok & 63) * 4);
      if (__all(msd != SENT)) break;
    }
    {
      const int col = 16 * dsl + 4 * g4, m8 = col / 80, cr = col - 80 * m8;
      hv = ld64(rs, WS_H + (((mytok >> 6) * 8 + m8) * 5 + (cr >> 4)) * 2048 + (mytok & 63) * 32 + (cr & 15) * 2);
    }
    v4u af[7];
    for (unsigned sp = 0; sp < SPIN_CAP; ++sp) {
      CBAR();
      bool ok = true;
#pragma unroll
      for (int i = 0; i < 7; ++i) {
        const int s = min(wave + 8 * i, 53);
        af[i] = ld128(rs, WS_ACT + s * 512 + g4 * 128 + (tk & 7) * 16);
      }
#pragma unroll
      for (int i = 0; i < 7; ++i) ok &= af[i].x != SENT && af[i].y != SENT && af[i].z != SENT && af[i].w != SENT;
      if (__all(ok)) break;
    }
    STAMP(18);
    WAITV(0); LBAR();
    f32x4 acc = {0, 0, 0, 0};
    lptr wb = lds + (8 * g4 + (tk >> 2)) * 32 + (tk & 3) * 8;
#pragma unroll
    for (int i = 0; i < 7; ++i) {
      const int s = wave + 8 * i;
      if (s < 54) acc = MFMA16(trread2(wb + s * 1024, 4 * 32), asbf(af[i]), acc);
    }
    *(LAS f32x4*)(lds + 57344 + wave * 1024 + lane * 16) = acc;
    LBAR();
    if (wave == 0 && tk < 8) {
      f32x4 y = {0, 0, 0, 0};
#pragma unroll
      for (int k = 0; k < 8; ++k) y += *(const LAS f32x4*)(lds + 57344 + k * 1024 + lane * 16);
      const unsigned ord = mykey >> 16, rbits = (ord & 0x8000u) ? (ord & 0x7FFFu) : (~ord & 0xFFFFu);
      const float r = __uint_as_float(rbits << 16), sg = rbf(1.f / (1.f + __expf(-r)));
      const float mb = lo16(msd), rsd = 1.f / hi16(msd);
      const float hh[4] = {lo16(hv.x), hi16(hv.x), lo16(hv.y), hi16(hv.y)};
      float o4[4];
#pragma unroll
      for (int e = 0; e < 4; ++e) { const float n = rbf(rbf(hh[e] - mb) * rsd), yy = rbf(rbf(y[e]) * sg); o4[e] = hh[e] + rbf(n + yy); }
      st64(ro, mytok * 1280 + (16 * dsl + 4 * g4) * 2, v2u{pk2(o4[0], o4[1]), pk2(o4[2], o4[3])});
    }
  }
  STAMP(19);
}

extern "C" void kernel_launch(void* const* d_in, const int* in_sizes, int n_in, void* d_out, int out_size, void* d_ws, size_t ws_size, hipStream_t stream) {
  (void)in_sizes; (void)n_in; (void)out_size; (void)ws_size;
  Params p;
  p.x = (const u16*)d_in[0]; p.wqkv = (const u16*)d_in[1]; p.wo = (const u16*)d_in[2];
  p.wr = (const u16*)d_in[4]; p.wg = (const u16*)d_in[5]; p.wu = (const u16*)d_in[6]; p.wd = (const u16*)d_in[7];
  p.out = (u16*)d_out; p.ws = (unsigned char*)d_ws;
  (void)hipFuncSetAttribute((const void*)mega_kernel, hipFuncAttributeMaxDynamicSharedMemorySize, LDS_BYTES);
  mega_kernel<<<256, 512, LDS_BYTES, stream>>>(p);
}
```

```cpp
#include <hip/hip_runtime.h>
#include <stdint.h>
#include <utility>
#include <type_traits>

#define PF2 1
#define GAS __attribute__((address_space(1)))
#define LAS __attribute__((address_space(3)))
#define DI __device__ __forceinline__
typedef unsigned short u16;
typedef unsigned long long u64;
typedef unsigned v4u __attribute__((ext_vector_type(4)));
typedef unsigned v2u __attribute__((ext_vector_type(2)));
typedef float f32x2 __attribute__((ext_vector_type(2)));
typedef float f32x4 __attribute__((ext_vector_type(4)));
typedef float f32x16 __attribute__((ext_vector_type(16)));
typedef __bf16 bf16x2 __attribute__((ext_vector_type(2)));
typedef __bf16 bf16x4 __attribute__((ext_vector_type(4)));
typedef __bf16 bf16x8 __attribute__((ext_vector_type(8)));
typedef GAS u64 gu64;
typedef GAS unsigned gu32;
typedef const LAS unsigned char* lptr;
typedef __amdgpu_buffer_rsrc_t rsrc_t;
#define RLX_AGENT __ATOMIC_RELAXED, __HIP_MEMORY_SCOPE_AGENT

constexpr int WS_EPOCH = 0;
constexpr int WS_QF = 8192;
constexpr int WS_OF = 40960;
constexpr int WS_TS = 77824;
constexpr int WS_CS = 94208;
constexpr int WS_S = 262144;
constexpr int WS_RK = 524288;
constexpr int WS_ACT = 557056;
constexpr int WS_QKV = 1 << 20;
constexpr int WS_OU = 9 << 20;
constexpr int WS_H = 12 << 20;
constexpr int WS_STAMP = 15 << 20;
constexpr int WS_BYTES = 16 << 20;

constexpr int L_MISC = 131072;
constexpr int L_K = 0, L_V = 30720;
constexpr int L_ZERO = L_K, L_BIG = L_V;
constexpr int L_WO = 61440;
constexpr int L_ONES = 158720;
constexpr int L_XT = 0;
constexpr int L_WLAST = 10240;
constexpr int L_RED = 15360;
constexpr int L_FW = 61440, L_FRED = 122880;
constexpr int L_ST = 131072, L_SEL = 131584;
constexpr int LDS_BYTES = 163840;

constexpr float QSCALE = 0.22360679774997896f * 1.4426950408889634f;
constexpr unsigned SENT = 0xFFFFFFFFu;
constexpr unsigned SPIN_CAP = 1u << 16;
constexpr int AUX_SC1 = 16;

#define CODE_BYTES 40960
struct Params { const u16* x; const u16* wqkv; const u16* wo; const u16* wr; const u16* wg; const u16* wu; const u16* wd; u16* out; unsigned char* ws; };

#define DLY 0
#define DELAY_AT(k) do { if (DLY == (k)) delay_ticks(300); if (DLY == 10 + (k)) delay_cycles(10000); } while (0)
#define WAITV(n) asm volatile("s_waitcnt vmcnt(" #n ")" ::: "memory")
#define WAITL(n) asm volatile("s_waitcnt lgkmcnt(" #n ")" ::: "memory")
#define BAR() __builtin_amdgcn_s_barrier()
#define CBAR() asm volatile("" ::: "memory")
#define LBAR() do { WAITL(0); BAR(); CBAR(); } while (0)
#define PIN(a) asm volatile("" : "+v"(a))
#define SGB(m, n) __builtin_amdgcn_sched_group_barrier(m, n, 0)
#define SB() __builtin_amdgcn_sched_barrier(0)
#define RFL(x) __builtin_amdgcn_readfirstlane(x)

#define STAMP(i)
#define CSTAMP(i)
#define STAMP_DUMP()
#define STAMP_INIT()

template <class F, int... I> DI void sfor_(F&& f, std::integer_sequence<int, I...>) { (f(std::integral_constant<int, I>{}), ...); }
template <int N, class F> DI void sfor(F&& f) { sfor_(f, std::make_integer_sequence<int, N>{}); }
DI unsigned pk2(float a, float b) { f32x2 v = {a, b}; bf16x2 r = __builtin_convertvector(v, bf16x2); return __builtin_bit_cast(unsigned, r); }
DI float rbf(float f) { return __uint_as_float(pk2(f, 0.f) << 16); }
DI float lo16(unsigned x) { return __uint_as_float(x << 16); }
DI float hi16(unsigned x) { return __uint_as_float(x & 0xFFFF0000u); }
DI float dot2(unsigned a, unsigned b, float c) { return __builtin_amdgcn_fdot2_f32_bf16(__builtin_bit_cast(bf16x2, a), __builtin_bit_cast(bf16x2, b), c, false); }
DI bf16x4 trread(lptr p) { return __builtin_amdgcn_ds_read_tr16_b64_v4bf16((LAS bf16x4*)p); }
DI bf16x8 trread2(lptr p, int off2) { bf16x4 a = trread(p), b = trread(p + off2); return __builtin_shufflevector(a, b, 0, 1, 2, 3, 4, 5, 6, 7); }
DI bf16x8 asbf(v4u v) { return __builtin_bit_cast(bf16x8, v); }
template <int CTRL> DI float dppf(float x) { return __builtin_bit_cast(float, __builtin_amdgcn_update_dpp(0, __builtin_bit_cast(int, x), CTRL, 0xF, 0xF, true)); }
template <int CTRL> DI unsigned dppu(unsigned x) { return (unsigned)__builtin_amdgcn_update_dpp(0, (int)x, CTRL, 0xF, 0xF, true); }
DI float rowsum16(float s) { s += dppf<0x128>(s); s += dppf<0x124>(s); s += dppf<0x122>(s); s += dppf<0x121>(s); return s; }
DI float xrowsum(float s) {
  auto a = __builtin_amdgcn_permlane16_swap(__float_as_uint(s), __float_as_uint(s), false, false);
  s = __uint_as_float(a[0]) + __uint_as_float(a[1]);
  auto b = __builtin_amdgcn_permlane32_swap(__float_as_uint(s), __float_as_uint(s), false, false);
  return __uint_as_float(b[0]) + __uint_as_float(b[1]);
}
DI unsigned san(unsigned x) { return x == SENT ? 0x7FC07FC0u : x; }
template <int OFF> DI void dma16(unsigned voff, const GAS void* sbase, unsigned ldsbase) {
  asm volatile("s_add_i32 m0, %2, %3\n\ts_nop 0\n\tglobal_load_lds_dwordx4 %0, %1" ::"v"(voff), "s"(sbase), "s"(ldsbase), "i"(OFF) : "memory", "m0", "scc");
}
template <int OFF> DI void dma16_sc1(unsigned voff, const GAS void* sbase, unsigned ldsbase) {
  asm volatile("s_add_i32 m0, %2, %3\n\ts_nop 0\n\tglobal_load_lds_dwordx4 %0, %1 sc1" ::"v"(voff), "s"(sbase), "s"(ldsbase), "i"(OFF) : "memory", "m0", "scc");
}
template <int OFF> DI void dma16_m(unsigned voff, const GAS void* sbase, unsigned ldsbase, u64 mask) {
  asm volatile("s_mov_b64 exec, %4\n\ts_add_i32 m0, %2, %3\n\ts_nop 0\n\tglobal_load_lds_dwordx4 %0, %1\n\ts_mov_b64 exec, -1" ::"v"(voff), "s"(sbase), "s"(ldsbase), "i"(OFF), "s"(mask) : "memory", "m0", "scc");
}
template <int OFF> DI void dma16_sc1_m(unsigned voff, const GAS void* sbase, unsigned ldsbase, u64 mask) {
  asm volatile("s_mov_b64 exec, %4\n\ts_add_i32 m0, %2, %3\n\ts_nop 0\n\tglobal_load_lds_dwordx4 %0, %1 sc1\n\ts_mov_b64 exec, -1" ::"v"(voff), "s"(sbase), "s"(ldsbase), "i"(OFF), "s"(mask) : "memory", "m0", "scc");
}
template <int OFF> DI void gld64(v2u& a, unsigned vo, const GAS void* sb) {
  asm volatile("global_load_dwordx2 %0, %1, %2 offset:%c3" : "=&v"(a) : "v"(vo), "s"(sb), "i"(OFF) : "memory");
}
DI void gtouch(const GAS void* ptr) { asm volatile("global_load_dword v255, %0, off" ::"v"(ptr) : "memory", "v255"); }
DI const GAS unsigned char* uni(const GAS unsigned char* q) { const u64 v = (u64)q; const unsigned lo = RFL((unsigned)v), hi = RFL((unsigned)(v >> 32)); return (const GAS unsigned char*)(((u64)hi << 32) | lo); }
DI void delay_ticks(unsigned n) { const u64 t0 = __builtin_amdgcn_s_memrealtime(); while (__builtin_amdgcn_s_memrealtime() - t0 < n) __builtin_amdgcn_s_sleep(1); }
DI void delay_cycles(unsigned n) { const u64 t0 = __builtin_amdgcn_s_memtime(); while (__builtin_amdgcn_s_memtime() - t0 < n) __builtin_amdgcn_s_sleep(1); }
template <int OFF> DI void dma16_u(unsigned voff, const GAS void* sbase, unsigned ldsbase) {
  asm volatile("s_add_i32 m0, %2, %3\n\ts_nop 4\n\tglobal_load_lds_dwordx4 %0, %1" ::"v"(voff), "s"(sbase), "s"(ldsbase), "i"(OFF) : "memory", "m0", "scc");
}
DI unsigned vmcnt_now() { const unsigned r = __builtin_amdgcn_s_getreg(7 | (23 << 11)); return (r & 15u) | ((r >> 18) & 48u); }
DI float rcp(float x) { return __builtin_amdgcn_rcpf(x); }
DI v4u ld128(rsrc_t rs, unsigned off) { return __builtin_amdgcn_raw_buffer_load_b128(rs, off, 0, AUX_SC1); }
DI v2u ld64(rsrc_t rs, unsigned off) { return __builtin_amdgcn_raw_buffer_load_b64(rs, off, 0, AUX_SC1); }
DI unsigned ld32(rsrc_t rs, unsigned off) { return __builtin_amdgcn_raw_buffer_load_b32(rs, off, 0, AUX_SC1); }
DI void st128(rsrc_t rs, unsigned off, v4u v) { __builtin_amdgcn_raw_buffer_store_b128(v, rs, off, 0, AUX_SC1); }
DI v4u ld128s(rsrc_t rs, unsigned voff, unsigned soff) { return __builtin_amdgcn_raw_buffer_load_b128(rs, voff, soff, AUX_SC1); }
DI unsigned umax(unsigned a, unsigned b) { return a > b ? a : b; }
DI unsigned umin(unsigned a, unsigned b) { return a < b ? a : b; }
DI unsigned umax3(unsigned a, unsigned b, unsigned c) { return umax(umax(a, b), c); }
DI void st64(rsrc_t rs, unsigned off, v2u v) { __builtin_amdgcn_raw_buffer_store_b64(v, rs, off, 0, AUX_SC1); }
DI void st32(rsrc_t rs, unsigned off, unsigned v) { __builtin_amdgcn_raw_buffer_store_b32(v, rs, off, 0, AUX_SC1); }
DI void poll_flags(rsrc_t rs, unsigned off, bool need, unsigned tlo, unsigned thi) {
  for (unsigned sp = 0; sp < SPIN_CAP; ++sp) {
    CBAR();
    v2u f = ld64(rs, off);
    if (__all(!need || (f.x == tlo && f.y == thi))) break;
    __builtin_amdgcn_s_sleep(1);
  }
}
#define MFMA32(a, b, c) __builtin_amdgcn_mfma_f32_32x32x16_bf16(a, b, c, 0, 0, 0)
#define MFMA16(a, b, c) __builtin_amdgcn_mfma_f32_16x16x32_bf16(a, b, c, 0, 0, 0)

DI void tlb_touch(u64 ws, u64 wo, u64 wg, u64 wu, u64 wd, u64 out, int idx) {
  u64 base = ws; int i = idx;
  if (idx >= 232) { base = wo; i = idx - 232; }
  if (idx >= 245) { base = wg; i = idx - 245; }
  if (idx >= 279) { base = wu; i = idx - 279; }
  if (idx >= 313) { base = wd; i = idx - 313; }
  if (idx >= 347) { base = out; i = idx - 347; }
  if (idx < 387) gtouch((const GAS void*)(base + (u64)i * 65536));
}

DI bool pf_line(u64 wg, u64 wu, u64 wd, int xcd, int li, const GAS unsigned char*& ptr) {
  const int ngu = xcd < 3 ? 4 : 3, ndn = xcd < 2 ? 2 : 1, nl_ = ngu * 1280 + ndn * 1728;
  const int l2 = li - ngu * 1280, isd = l2 >= 0;
  const int lg = isd ? l2 : li, j = isd ? (lg >= 1728) : lg / 1280, r = lg - j * (isd ? 1728 : 1280), mat = !isd && r >= 640;
  const u64 b_ = isd ? wd : (mat ? wu : wg);
  ptr = (const GAS unsigned char*)(b_ + (unsigned)((r - (mat ? 640 : 0)) * (isd ? 1280 : 3456) + (xcd + 8 * j) * 128));
  return li < nl_;
}

extern "C" __global__ void __launch_bounds__(512, 2) mega_kernel(Params p_) {
  if (__builtin_amdgcn_readfirstlane(threadIdx.x >> 6) == 6) {
    u64 pc; unsigned off;
    asm volatile("s_getpc_b64 %0\n.Lpfh%=:\n\ts_mov_b32 %1, .Lpfh%=-mega_kernel" : "=&s"(pc), "=&s"(off));
    const unsigned lines = CODE_BYTES >> 7, per = (lines + 31) >> 5, ln = threadIdx.x & 63, l = (blockIdx.x >> 3) * per + ln;
    if (ln < per && l < lines) gtouch((const GAS unsigned char*)(pc - off) + l * 128);
  }
  u64 a0_ = (u64)p_.x, a1_ = (u64)p_.wqkv, a2_ = (u64)p_.wo, a3_ = (u64)p_.wr, a4_ = (u64)p_.wg, a5_ = (u64)p_.wu, a6_ = (u64)p_.wd, a7_ = (u64)p_.out, a8_ = (u64)p_.ws;
  asm volatile("" : "+s"(a0_), "+s"(a1_), "+s"(a2_), "+s"(a3_), "+s"(a4_), "+s"(a5_), "+s"(a6_), "+s"(a7_), "+s"(a8_));
  const u16* const P_x = (const u16*)a0_; const u16* const P_wqkv = (const u16*)a1_; const u16* const P_wo = (const u16*)a2_; const u16* const P_wr = (const u16*)a3_;
  const u16* const P_wg = (const u16*)a4_; const u16* const P_wu = (const u16*)a5_; const u16* const P_wd = (const u16*)a6_;
  u16* const P_out = (u16*)a7_; unsigned char* const P_ws = (unsigned char*)a8_;
  extern __shared__ __attribute__((aligned(16))) unsigned char lds_[];
  LAS unsigned char* lds = (LAS unsigned char*)lds_;
  const unsigned lds0 = (unsigned)(size_t)lds;
  const int wg = blockIdx.x, tid = threadIdx.x, lane = tid & 63, wave = RFL(tid >> 6);
  const int xcd = wg & 7, loc = wg >> 3;
  GAS unsigned char* ws = (GAS unsigned char*)P_ws;
  const rsrc_t rs = __builtin_amdgcn_make_buffer_rsrc((void*)P_ws, 0, WS_BYTES, 0x00020000);
  const int h = lane >> 5, t = lane & 31;
  STAMP_INIT();
  STAMP(0);
  DELAY_AT(0 + 6);
#define PF2_AT 0
  const int rb = 4 * xcd + (loc >> 3), mm = loc & 7;
  const bool leader = (mm == 7);
  const int nl = loc - (loc >> 3);
  const int fq = xcd + 8 * (nl >> 2);
  const bool is_gu = !leader && nl < 16 && fq < 27;  const int fsl = 4 * fq + (nl & 3);
  const int dq = xcd + 8 * ((nl - 16) >> 2);
  const bool is_dn = !leader && nl >= 16 && nl < 24 && dq < 10;  const int dsl = 4 * dq + (nl & 3);
  unsigned tlo, thi;
#define GET_T(e) do { const u64 T_ = ((((u64)(e).y) << 32) | (e).x) + 1; tlo = RFL((unsigned)T_); thi = RFL((unsigned)(T_ >> 32)); } while (0)

  const int hpB = 2 * xcd + (loc & 1), tbB = loc >> 1;
  auto clear_mailboxes = [&]() {
    const v4u s4 = {SENT, SENT, SENT, SENT};
#pragma unroll
    for (int r = 0; r < 3; ++r) {
      const int n = wave + 8 * r, i = n < 10 ? n : n - 10;
      const int u = 5 * hpB + (i >> 1), tok = tbB * 128 + (i & 1) * 64 + lane;
      unsigned a = n < 10 ? WS_OU + (tok >> 4) * 20480 + (u >> 2) * 1024 + (u & 3) * 256 + (tok & 15) * 16 : WS_H + (rb * 8 + mm) * 10240 + i * 1024 + lane * 16;
      if (n == 20) a = WS_S + ((2 * tbB + (hpB >> 3)) * 8 + (hpB & 7)) * 1024 + lane * 16;
      if (n == 21) a = WS_ACT + fsl * 256 + lane * 16;
      if (n < 21 || (n == 21 && is_gu && lane < 16)) st128(rs, a, s4);
    }
  };

  const GAS unsigned char* Xb = (const GAS unsigned char*)P_x;
  int a_tb = 0, a_cb = 0;
  if (__builtin_expect(loc >= 30, 0)) {
    const int tb = xcd * 2 + (loc - 30);
    const GAS unsigned char* xb = Xb + (size_t)(tb * 128 + wave * 16 + (lane >> 4)) * 1280 + (lane & 15) * 16;
    v4u v[4][5];
#pragma unroll
    for (int g = 0; g < 4; ++g)
#pragma unroll
      for (int j = 0; j < 5; ++j) v[g][j] = *(const GAS v4u*)(xb + g * 4 * 1280 + j * 256);
    const v2u e = ld64(rs, WS_EPOCH);
    SB();
    GET_T(e);
    const unsigned ONES = 0x3F803F80u;
#pragma unroll
    for (int g = 0; g < 4; ++g) {
      float s = 0.f, q = 0.f;
#pragma unroll
      for (int j = 0; j < 5; ++j)
#pragma unroll
        for (int c = 0; c < 4; ++c) { s = dot2(v[g][j][c], ONES, s); q = dot2(v[g][j][c], v[g][j][c], q); }
      s = rowsum16(s); q = rowsum16(q);
      const float mean = s * (1.f / 640.f), var = fmaxf(q * (1.f / 640.f) - mean * mean, 0.f);
      const float mb = rbf(mean), sd = rbf(sqrtf(rbf(rbf(var) + 1e-5f)));
      if ((lane & 15) == 0) st64(rs, WS_TS + (tb * 128 + wave * 16 + g * 4 + (lane >> 4)) * 8, v2u{(__float_as_uint(mb) >> 16) | (__float_as_uint(sd) & 0xFFFF0000u), tlo});
    }
    {
      const int hi_ = xcd * 2 + (loc - 30);
      for (int l = hi_ * 400 + tid; l < (hi_ + 1) * 400; l += 512) gtouch((const GAS unsigned char*)P_wo + l * 128);
    }
  } else {
    const int ti = xcd * 30 + loc, ci = ti / 5, cbi = ti - 5 * ci, tb = ci & 15, cb = (ci >> 4) * 5 + cbi;
#define ROT 2
    const int c0 = ROT == 0 ? 0 : (ROT == 1 ? (tb + 2 * cb) % 10 : (ROT == 2 ? (cbi & 1) * 5 : (ROT == 3 ? (cbi + ci % 6) % 7 : (cbi + ci % 6) % 8)));
    LAS float* stm = (LAS float*)(lds + L_MISC); LAS float* str = stm + 128; LAS float* csp = stm + 256;
    if (__builtin_expect(wave == 4 || wave == 5, 1)) {
      const int lw = wave - 4;
      const GAS unsigned char* Xg = uni(Xb + (size_t)tb * 128 * 1280);
      const GAS unsigned char* Wg = uni((const GAS unsigned char*)P_wqkv + cb * 256);
      unsigned xoff[8], woff[8];
      {
        const unsigned x0 = (lw * 64 + (lane >> 3)) * 1280, w0 = (lw * 32 + (lane >> 4)) * 3840 + (((lane & 15) ^ (4 * ((lane >> 4) & 3))) << 4);
        const unsigned sa = ((lane & 7) ^ (lane >> 4)) << 4, sb2 = ((lane & 7) ^ (4 + (lane >> 4))) << 4;
#pragma unroll
        for (int i = 0; i < 8; ++i) { xoff[i] = x0 + i * 8 * 1280 + ((i & 1) ? sb2 : sa); woff[i] = w0 + i * 4 * 3840; asm volatile("" : "+v"(xoff[i]), "+v"(woff[i])); }
      }
      auto issue = [&](int ci_) {
        int c = ci_ + c0; c = c >= 10 ? c - 10 : c;
        const unsigned dst = lds0 + (ci_ & 3) * 32768 + lw * 8192;
        const GAS unsigned char* xb = Xg + c * 128; const GAS unsigned char* wb = Wg + (size_t)c * 64 * 3840;
        sfor<8>([&](auto I) { constexpr int i = I; dma16<i * 1024>(xoff[i], xb, dst); dma16<16384 + i * 1024>(woff[i], wb, dst); });
      };
      STAMP(23);
#pragma unroll 1
      for (int c = 0; c < 10; ++c) {
        if (c >= 3) { WAITV(32); BAR(); }
        issue(c);
      }
      WAITV(32); BAR();
      WAITV(16); BAR();
      WAITV(0); BAR();
      const v2u e = ld64(rs, WS_EPOCH);
      GET_T(e);
      LBAR();
    } else if (wave >= 6) {
      const int cw = wave - 6;
      if (tb < 10) {
        const GAS unsigned char* wsrc = (const GAS unsigned char*)P_wqkv + cb * 256 + (size_t)(tb * 64 + cw * 32 + (lane >> 4)) * 3840 + (lane & 15) * 16;
        v4u w[8];
#pragma unroll
        for (int i = 0; i < 8; ++i) w[i] = *(const GAS v4u*)(wsrc + (size_t)i * 4 * 3840);
        unsigned sel0 = 0x00003F80u, sel1 = 0x3F800000u;
        asm volatile("" : "+v"(sel0), "+v"(sel1));
        float cs[8] = {0, 0, 0, 0, 0, 0, 0, 0};
#pragma unroll
        for (int i = 0; i < 8; ++i)
#pragma unroll
          for (int e_ = 0; e_ < 4; ++e_) { cs[2 * e_] = dot2(w[i][e_], sel0, cs[2 * e_]); cs[2 * e_ + 1] = dot2(w[i][e_], sel1, cs[2 * e_ + 1]); }
        LAS float* dst = csp + (cw * 4 + (lane >> 4)) * 128 + (lane & 15) * 8;
        *(LAS f32x4*)dst = f32x4{cs[0], cs[1], cs[2], cs[3]}; *(LAS f32x4*)(dst + 4) = f32x4{cs[4], cs[5], cs[6], cs[7]};
      }
      const v2u e = ld64(rs, WS_EPOCH);
      WAITL(0);
      BAR();
      CBAR();
      float csum = 0.f;
      if (tb < 10) {
#pragma unroll
        for (int g = 0; g < 8; ++g) csum += csp[g * 128 + cw * 64 + lane];
        PIN(csum);
      }
      BAR();
      GET_T(e);
      if (tb < 10) st64(rs, WS_CS + (cb * 10 + tb) * 1024 + (cw * 64 + lane) * 8, v2u{__float_as_uint(csum), tlo});
      BAR();
      BAR();
      BAR();
      BAR();
      v2u g0 = {0, 0}, g1 = {0, 0}; v4u pp[10];
      auto fetch = [&]() {
        if (cw == 0) { g0 = ld64(rs, WS_TS + (tb * 128 + lane) * 8); g1 = ld64(rs, WS_TS + (tb * 128 + 64 + lane) * 8); }
        else {
#pragma unroll
          for (int k = 0; k < 10; ++k) pp[k] = ld128(rs, WS_CS + (cb * 10 + k) * 1024 + lane * 16);
        }
      };
      auto valid = [&]() {
        bool ok = true;
        if (cw == 0) ok = g0.y == tlo && g1.y == tlo;
        else {
#pragma unroll
          for (int k = 0; k < 10; ++k) ok &= (pp[k].y == tlo) && (pp[k].w == tlo);
        }
        return (bool)__all(ok);
      };
      auto put = [&]() {
        if (cw == 0) { *(LAS f32x2*)(stm + 2 * lane) = f32x2{-lo16(g0.x), rcp(hi16(g0.x))}; *(LAS f32x2*)(stm + 128 + 2 * lane) = f32x2{-lo16(g1.x), rcp(hi16(g1.x))}; }
        else {
          float a = 0.f, b = 0.f;
#pragma unroll
          for (int k = 0; k < 10; ++k) { a += __uint_as_float(pp[k].x); b += __uint_as_float(pp[k].z); }
          *(LAS f32x2*)(csp + 16 * 128 + 2 * lane) = f32x2{a, b};
        }
      };
      fetch();
      BAR();
      BAR();
      bool done = valid();
      if (!done) { CBAR(); fetch(); }
      BAR();
      if (done) put();
      BAR();
      if (!done) {
        for (unsigned sp = 0; sp < SPIN_CAP; ++sp) { if (valid()) break; CBAR(); fetch(); }
        put();
      }
      LBAR();
    } else {
      const v2u e = ld64(rs, WS_EPOCH);
      const int th = wave & 1, tp = wave >> 1;
      unsigned xa[4];
#pragma unroll
      for (int s = 0; s < 4; ++s) xa[s] = (64 * th + t) * 128 + (((2 * s + h) ^ ((t >> 1) & 7)) << 4);
      const int G = lane >> 4, i16 = lane & 15, q = i16 >> 2, pp = i16 & 3, hh = G >> 1, fb = G & 1;
      unsigned wa[2];
#pragma unroll
      for (int j = 0; j < 2; ++j) wa[j] = 16384 + (8 * hh + q) * 256 + (((((tp * 2 + j) ^ q) << 2) | (fb << 1) | (pp >> 1)) << 4) + (pp & 1) * 8;
      f32x16 acc[2][2];
#pragma unroll
      for (int i = 0; i < 2; ++i)
#pragma unroll
        for (int j = 0; j < 2; ++j)
#pragma unroll
          for (int r = 0; r < 16; ++r) acc[i][j][r] = 0.f;
      v4u xf[2][2]; bf16x8 wf[2][2];
      auto ld = [&](int b, lptr sb, int s) {
        xf[b][0] = *(const LAS v4u*)(sb + xa[s]); xf[b][1] = *(const LAS v4u*)(sb + xa[s] + 4096);
#pragma unroll
        for (int j = 0; j < 2; ++j) wf[b][j] = trread2(sb + wa[j] + (16 * s) * 256, 1024);
      };
      auto mma = [&](int b) {
#pragma unroll
        for (int j = 0; j < 2; ++j)
#pragma unroll
          for (int i = 0; i < 2; ++i) acc[i][j] = MFMA32(wf[b][j], asbf(xf[b][i]), acc[i][j]);
      };
#define GRP() SGB(0x8, 1); SGB(0x100, 2); SGB(0x8, 1); SGB(0x100, 2); SGB(0x8, 1); SGB(0x100, 2); SGB(0x8, 1); SB()
      BAR();
      CBAR();
      STAMP(1); CSTAMP(24);
      ld(0, lds, 0);
#define UNR 10
#pragma unroll UNR
      for (int c = 0; c < 10; ++c) {
        lptr sb = lds + (c & 3) * 32768;
        ld(1, sb, 1); mma(0); GRP();
        ld(0, sb, 2); mma(1); GRP();
        ld(1, sb, 3); mma(0); GRP();
        if (c < 9) { WAITL(0); BAR(); CBAR(); }
        ld(0, lds + ((c + 1) & 3) * 32768, 0);
        mma(1); GRP();
      }
      STAMP(2); CSTAMP(25);
      GET_T(e);
      LBAR();
#define VDELAY 0
      if (VDELAY > 0 && cb >= 10) __builtin_amdgcn_s_sleep(VDELAY);
      const float qs = cb < 5 ? QSCALE : 1.f;
      const LAS float* cs = csp + 16 * 128;
      f32x2 st2[2]; f32x4 c4[2][4];
#pragma unroll
      for (int i = 0; i < 2; ++i) st2[i] = *(const LAS f32x2*)(stm + 2 * ((2 * th + i) * 32 + t));
#pragma unroll
      for (int j = 0; j < 2; ++j)
#pragma unroll
        for (int g = 0; g < 4; ++g) c4[j][g] = *(const LAS f32x4*)(cs + (tp * 2 + j) * 32 + 8 * g + 4 * h);
      const unsigned sv = WS_QKV + h * 2048 + (2 * th * 32 + t) * 16, so = (tb * 240 + cb * 16 + tp * 8) * 2048;
      SB();
#pragma unroll
      for (int j = 0; j < 2; ++j)
#pragma unroll
        for (int i = 0; i < 2; ++i) {
          const f32x2 nm2 = {st2[i][0], st2[i][0]}, rs2 = {st2[i][1] * qs, st2[i][1] * qs};
          unsigned P[4][2];
#pragma unroll
          for (int g = 0; g < 4; ++g) {
            f32x2 a = {acc[i][j][4 * g], acc[i][j][4 * g + 1]}, b = {acc[i][j][4 * g + 2], acc[i][j][4 * g + 3]};
            const f32x2 ca = {c4[j][g][0], c4[j][g][1]}, cb2 = {c4[j][g][2], c4[j][g][3]};
            a = (ca * nm2 + a) * rs2; b = (cb2 * nm2 + b) * rs2;
            P[g][0] = pk2(a[0], a[1]); P[g][1] = pk2(b[0], b[1]);
          }
#pragma unroll
          for (int gp = 0; gp < 2; ++gp) {
            auto r0 = __builtin_amdgcn_permlane32_swap(P[2 * gp][0], P[2 * gp + 1][0], false, false);
            auto r1 = __builtin_amdgcn_permlane32_swap(P[2 * gp][1], P[2 * gp + 1][1], false, false);
            st128(rs, sv + so + (j * 4 + 2 * gp) * 2048 + i * 512, v4u{r0[0], r1[0], r0[1], r1[1]});
          }
        }
      STAMP(3);
    }
    a_tb = tb; a_cb = cb;
  }
  if (loc < 30) {
    WAITV(0);
    BAR();
    CBAR();
    if (tid == 0) st64(rs, WS_QF + (a_tb * 15 + a_cb) * 128, v2u{tlo, thi});
  }
  STAMP(4);

  const int rt = wave & 3, kh = wave >> 2, g4 = lane >> 4, tk = lane & 15;
  v2u wr4[5];
  unsigned wrow = 0, wvoff = 0;
  auto wo_setup = [&]() {
    const int G4 = wave + 8 * (lane < 20 ? lane : 0), s_ = G4 >> 3, r = G4 & 7, g = ((r >> 2) << 1) | (r & 1), t2 = (r >> 1) & 1;
    const int u = 4 * s_ + g, hq = u / 5, j5 = u - 5 * hq;
    wrow = (40 * hq + (j5 == 4 ? (t2 ? 36 : 16) : ((j5 >> 1) * 20 + (j5 & 1) * 8 + 4 * t2))) * 1280;
    wvoff = (lane / 10) * 1280 + (lane % 10) * 16;
  };
  const GAS unsigned char* wob = (const GAS unsigned char*)P_wo + mm * 160;
#define WO_EARLY 10
#define WO_DMA(k) dma16_m<((k) < 19 ? L_WO + (k) * 5120 : L_WLAST)>(wvoff, wob + (unsigned)__builtin_amdgcn_readlane((int)wrow, (k)), lds0 + wave * 640, 0xFFFFFFFFFFull)
  {
    const int hp = hpB, tb = tbB;
    const int a = wave >> 2, j4 = wave & 3;
    const int i0 = max(0, 8 - 4 * tb - j4);
    DELAY_AT(1);
    if (tb < 2) {
      const int nk = 256 - 128 * tb;
      for (int idx = tid; idx < 10 * nk; idx += 512) { const int u = idx / nk; *(LAS v4u*)(lds + u * 6144 + (idx - u * nk) * 16) = v4u{0, 0, 0, 0}; }
      LBAR();
    }
    const GAS unsigned char* qkv = uni(ws + WS_QKV);
    unsigned kvo[4], vvo[4], kdst[4]; u64 kmask[4], vmask[4];
#pragma unroll
    for (int i = 0; i < 4; ++i) {
      int n = wave + 8 * i; n = n < 30 ? n : n - 8;
      const int u = n / 6, r = n - 6 * u, hf = r & 1; int j = r >> 1; j = tb - 2 + j >= 0 ? j : 2;
      const unsigned po = (((tb - 2 + j) * 240 + 80 + 5 * hp + u) * 128 + hf * 64) * 16;
      kvo[i] = po + lane * 16;
      vvo[i] = po + 80 * 2048 + ((lane & 48) | ((((lane >> 2) - u) & 3) << 2) | (lane & 3)) * 16;
      kdst[i] = lds0 + u * 6144 + j * 2048 + hf * 1024;
      kmask[i] = (j == 0 && hf == 0) ? ~1ull : ~0ull; vmask[i] = (j == 0 && hf == 0 && u == 0) ? ~1ull : ~0ull;
      PIN(kvo[i]); PIN(vvo[i]);
      asm volatile("" : "+s"(kdst[i]), "+s"(kmask[i]), "+s"(vmask[i]));
    }
    unsigned qa0 = WS_QKV + ((tb * 240 + 5 * hp + (a ? 3 + h : h)) * 128 + j4 * 32 + t) * 16, qa1 = WS_QKV + ((tb * 240 + 5 * hp + 2) * 128 + j4 * 32 + t) * 16;
    PIN(qa0); PIN(qa1);
    if (wave == 0) {
      const int cq0 = (5 * hp) >> 4, cq1 = (5 * hp + 4) >> 4;
      const int fthird = lane < 2 ? 0 : (lane < 8 ? 1 : 2), ftb = tb - (lane < 2 ? 0 : ((lane - 2) % 6) >> 1);
      const unsigned foff = WS_QF + ((ftb >= 0 ? ftb : 0) * 15 + fthird * 5 + ((lane & 1) ? cq1 : cq0)) * 128;
      poll_flags(rs, foff, lane < 14 && ftb >= 0, tlo, thi);
    } else {
      if (wave == 1 && lane < 6) *(LAS v4u*)(lds + L_K + lane * 6144) = v4u{lane == 5 ? 0x0000F14Au : 0u, 0u, 0u, 0u};
      if (wave >= 3) *(LAS v4u*)(lds + L_ONES + (tid - 192) * 16) = v4u{0x00003F80u, 0u, 0x00003F80u, 0u};
      wo_setup();
      sfor<WO_EARLY>([&](auto I) { constexpr int k = I; WO_DMA(k); });
    }
    LBAR();
    STAMP(5);
    v4u q0, q1;
#pragma unroll
    for (int i = 0; i < 4; ++i) dma16_sc1_m<L_K>(kvo[i], qkv, kdst[i], kmask[i]);
    asm volatile("buffer_load_dwordx4 %0, %2, %4, 0 offen sc1\n\tbuffer_load_dwordx4 %1, %3, %4, 0 offen sc1" : "=&v"(q0), "=&v"(q1) : "v"(qa0), "v"(qa1), "s"(rs) : "memory");
#pragma unroll
    for (int i = 0; i < 4; ++i) dma16_sc1_m<L_V>(vvo[i], qkv, kdst[i], vmask[i]);
    if (wave == 0) wo_setup();
    f32x16 mD, cc, zero;
#pragma unroll
    for (int r = 0; r < 16; ++r) { const int kl = (r & 3) + 8 * (r >> 2) + 4 * h; mD[r] = kl <= t ? 0.f : -1e30f; cc[r] = kl <= t ? -1e30f : 0.f; zero[r] = 0.f; }
    PIN(mD); PIN(cc);
    lptr kp0 = lds + L_K + (a ? 3 + h : h) * 6144 + (j4 * 32 + t) * 16;
    unsigned ka1[9];
#pragma unroll
    for (int i = 0; i < 9; ++i) { ka1[i] = h ? (i < i0 ? L_BIG : L_ZERO) : L_K + 2 * 6144 + (j4 * 32 + t) * 16 + i * 512; PIN(ka1[i]); }
    unsigned vpo[2];
    {
      const int G = lane >> 4, q = (lane & 15) >> 2, pp = lane & 3, hh = G >> 1, c = 4 * (G & 1) + pp;
      const int gc = a ? 5 + c : c, u = gc >> 1;
#pragma unroll
      for (int t2 = 0; t2 < 2; ++t2) {
        const int kg = hh + 2 * t2;
        vpo[t2] = c < 5 ? L_V + u * 6144 + j4 * 512 + (((kg + u) & 3) << 6) + q * 16 + (gc & 1) * 8 : L_ONES + (((kg + (a ? 1 : 3)) & 3) << 6) + q * 16;
        PIN(vpo[t2]);
      }
    }
    const lptr vp[2] = {lds + vpo[0], lds + vpo[1]};
    asm volatile("s_waitcnt vmcnt(4)" : "+v"(q0), "+v"(q1)::"memory");
    LBAR();
    clear_mailboxes();
    STAMP(6); CSTAMP(28);
    v4u K0[9], K1[9];
    auto ldk = [&](auto I) { constexpr int i = I; K0[i] = *(const LAS v4u*)(kp0 + i * 512); K1[i] = *(const LAS v4u*)(lds + ka1[i]); };
    sfor<4>(ldk);
    q1 = h ? v4u{0x00003F80u, 0, 0, 0} : (a ? v4u{0, 0, q1.z, q1.w} : v4u{q1.x, q1.y, 0, 0});
    const bf16x8 Q0 = asbf(q0), Q1 = asbf(q1);
    f32x16 S[3];
    float mA = -INFINITY, mB = -INFINITY, m;
    S[0] = MFMA32(asbf(K1[0]), Q1, MFMA32(asbf(K0[0]), Q0, cc));
    S[1] = MFMA32(asbf(K1[1]), Q1, MFMA32(asbf(K0[1]), Q0, zero));
    SB();
    sfor<9>([&](auto I) {
      constexpr int i = I;
      if constexpr (i + 4 < 9) ldk(std::integral_constant<int, i + 4>{});
      if constexpr (i < 7) S[(i + 2) % 3] = MFMA32(asbf(K0[i + 2]), Q0, i + 2 == 8 ? mD : zero);
      SB();
#pragma unroll
      for (int r = 0; r < 8; r += 2) mA = __builtin_fmaxf(__builtin_fmaxf(mA, S[i % 3][r]), S[i % 3][r + 1]);
      PIN(mA);
      SB();
      if constexpr (i < 7) S[(i + 2) % 3] = MFMA32(asbf(K1[i + 2]), Q1, S[(i + 2) % 3]);
      SB();
#pragma unroll
      for (int r = 8; r < 16; r += 2) mB = __builtin_fmaxf(__builtin_fmaxf(mB, S[i % 3][r]), S[i % 3][r + 1]);
      PIN(mB);
      if constexpr (WO_EARLY + i < 19) WO_DMA(WO_EARLY + i);
      SB();
    });
    STAMP(20); CSTAMP(29);
    m = __builtin_fmaxf(mA, mB);
    { auto r = __builtin_amdgcn_permlane32_swap(__float_as_uint(m), __float_as_uint(m), false, false); m = __builtin_fmaxf(__uint_as_float(r[0]), __uint_as_float(r[1])); }
    f32x16 O = zero, cN;
#pragma unroll
    for (int r = 0; r < 16; ++r) { cN[r] = -m; cc[r] -= m; }
    S[0] = MFMA32(asbf(K1[0]), Q1, MFMA32(asbf(K0[0]), Q0, cc));
#pragma unroll
    for (int r = 0; r < 16; ++r) cc[r] = mD[r] - m;
    WAITV(0);
    LBAR();
    STAMP(21); CSTAMP(26);
    bf16x8 Vlo[2], Vhi[2];
    auto ldv = [&](int i, int s2) { return __builtin_shufflevector(trread(vp[0] + i * 512 + s2 * 256), trread(vp[1] + i * 512 + s2 * 256), 0, 1, 2, 3, 4, 5, 6, 7); };
    Vlo[0] = ldv(0, 0);
    const GAS unsigned char* pf_ptr; const bool pf_on = lane < 34 && pf_line(a4_, a5_, a6_, xcd, (loc * 8 + wave) * 34 + lane, pf_ptr);
    unsigned Ph[4]; float e12 = 0.f, e13 = 0.f, e14 = 0.f, e15 = 0.f;
#define EX(v) __builtin_amdgcn_exp2f(v)
    SB();
    sfor<9>([&](auto I) {
      constexpr int i = I;
      const f32x16& s = S[i & 1];
      unsigned Pl[4];
      float a0 = EX(s[0]), a1 = EX(s[1]), a2 = EX(s[2]), a3 = EX(s[3]);
      if constexpr (i > 0) { Ph[2] = pk2(e12, e13); Ph[3] = pk2(e14, e15); }
      SB();
      if constexpr (i < 8) S[(i + 1) & 1] = MFMA32(asbf(K0[i + 1]), Q0, i + 1 == 8 ? cc : cN);
      SB();
      float a4 = EX(s[4]), a5 = EX(s[5]), a6 = EX(s[6]), a7 = EX(s[7]);
      Pl[0] = pk2(a0, a1); Pl[1] = pk2(a2, a3);
      SB();
      if constexpr (i > 0) O = MFMA32(Vhi[(i - 1) & 1], asbf(v4u{Ph[0], Ph[1], Ph[2], Ph[3]}), O);
      if constexpr (i >= 4 && WO_EARLY + 9 + 2 * (i - 4) < 19) WO_DMA(WO_EARLY + 9 + 2 * (i - 4));
      SB();
      float a8 = EX(s[8]), a9 = EX(s[9]), a10 = EX(s[10]), a11 = EX(s[11]);
      Pl[2] = pk2(a4, a5); Pl[3] = pk2(a6, a7);
      SB();
      if constexpr (i < 8) S[(i + 1) & 1] = MFMA32(asbf(K1[i + 1]), Q1, S[(i + 1) & 1]);
      if constexpr (i >= 4 && WO_EARLY + 10 + 2 * (i - 4) < 19) WO_DMA(WO_EARLY + 10 + 2 * (i - 4));
      SB();
      e12 = EX(s[12]); e13 = EX(s[13]); e14 = EX(s[14]); e15 = EX(s[15]);
      Ph[0] = pk2(a8, a9); Ph[1] = pk2(a10, a11);
      SB();
      O = MFMA32(Vlo[i & 1], asbf(v4u{Pl[0], Pl[1], Pl[2], Pl[3]}), O);
      if constexpr (i == PF2_AT) { if (pf_on) gtouch(pf_ptr); }
      PIN(e12); PIN(e13); PIN(e14); PIN(e15);
      SB();
      Vhi[i & 1] = ldv(i, 1);
      if constexpr (i < 8) Vlo[(i + 1) & 1] = ldv(i + 1, 0);
      SB();
    });
    Ph[2] = pk2(e12, e13); Ph[3] = pk2(e14, e15);
    O = MFMA32(Vhi[0], asbf(v4u{Ph[0], Ph[1], Ph[2], Ph[3]}), O);

    STAMP(7); CSTAMP(27);
    DELAY_AT(2);
    {
      auto lr = __builtin_amdgcn_permlane32_swap(__float_as_uint(O[8]), __float_as_uint(O[8]), false, false);
      const float li = rcp(__uint_as_float(lr[1]));
      unsigned G0[2], G1[2], G2[2];
      G0[0] = san(pk2(O[0] * li, O[1] * li)); G0[1] = san(pk2(O[2] * li, O[3] * li));
      G1[0] = san(pk2(O[4] * li, O[5] * li)); G1[1] = san(pk2(O[6] * li, O[7] * li));
      G2[0] = san(pk2(O[8] * li, O[9] * li)); G2[1] = san(pk2(O[10] * li, O[11] * li));
      auto r0 = __builtin_amdgcn_permlane32_swap(G0[0], G1[0], false, false);
      auto r1 = __builtin_amdgcn_permlane32_swap(G0[1], G1[1], false, false);
      const int tok = tb * 128 + j4 * 32 + t;
      const unsigned ob = WS_OU + (tok >> 4) * 20480 + (tok & 15) * 16;
      const int u = 5 * hp + 2 * a + h, u4 = 5 * hp + 4;
      st128(rs, ob + (u >> 2) * 1024 + (u & 3) * 256, v4u{r0[0], r1[0], r0[1], r1[1]});
      if (h == 0) st64(rs, ob + (u4 >> 2) * 1024 + (u4 & 3) * 256 + a * 8, v2u{G2[0], G2[1]});
    }
    LBAR();
    if (tid == 0) st64(rs, WS_OF + (tb * 16 + hp) * 128, v2u{tlo, thi});
    {
      const GAS unsigned char* xp = Xb + (size_t)(64 * rb + 8 * wave) * 1280 + mm * 160;
      dma16_m<L_XT>(wvoff, xp, lds0 + wave * 1280, 0xFFFFFFFFFFull);
      dma16_m<L_XT + 640>(wvoff, xp + 4 * 1280, lds0 + wave * 1280, 0xFFFFFFFFFFull);
      WO_DMA(19);
      if (wave == 0) sfor<WO_EARLY>([&](auto I) { constexpr int k = I; WO_DMA(k); });
      const unsigned wo_ = (80 * mm + 4 * g4) * 2;
      sfor<5>([&](auto I) { constexpr int ct = I; gld64<ct * 32>(wr4[ct], wo_, (const GAS void*)P_wr); });
    }
    STAMP(8);
  }

  unsigned hp2[5][2];
  float wsum = 0.f;
  {
    if (wave == 4) {
      if (leader) {
        const GAS unsigned* wr32 = (const GAS unsigned*)P_wr;
#pragma unroll
        for (int k = 0; k < 5; ++k) { const unsigned w2 = wr32[k * 64 + lane]; wsum += lo16(w2) + hi16(w2); }
        wsum = xrowsum(rowsum16(wsum));
      }
      poll_flags(rs, WS_OF + ((rb >> 1) * 16 + (lane & 15)) * 128, lane < 16, tlo, thi);
    }
    asm volatile("s_waitcnt vmcnt(0)" : "+v"(wr4[0]), "+v"(wr4[1]), "+v"(wr4[2]), "+v"(wr4[3]), "+v"(wr4[4])::"memory");
    BAR(); CBAR();
    STAMP(9);
    const unsigned ov = WS_OU + lane * 16, os = (4 * rb + rt) * 20480 + 10 * kh * 1024;
    const unsigned wlane = (16 * (g4 >> 1) + 4 * (g4 & 1) + (tk >> 2)) * 160 + (tk & 3) * 8;
    lptr wb = lds + L_WO + kh * 320 * 160 + wlane;
    lptr wl = kh ? lds + L_WLAST + wlane : wb + 9 * 32 * 160;
    f32x4 acc[5];
    v4u of[10];
#pragma unroll
    for (int s = 0; s < 10; ++s) of[s] = ld128s(rs, ov + s * 1024, os);
    bf16x8 wf[2][5];
#pragma unroll
    for (int ct = 0; ct < 5; ++ct) { wf[0][ct] = trread2(wb + ct * 32, 8 * 160); acc[ct] = f32x4{0, 0, 0, 0}; }
    SB();
    sfor<10>([&](auto I) {
      constexpr int s = I;
      if (__builtin_expect(__any(umax3(of[s].x, of[s].y, of[s].z) == SENT || of[s].w == SENT), 0)) {
        for (unsigned sp = 0; sp < SPIN_CAP; ++sp) {
          CBAR();
          of[s] = ld128s(rs, ov + s * 1024, os);
          if (!__any(umax3(of[s].x, of[s].y, of[s].z) == SENT || of[s].w == SENT)) break;
        }
      }
      SB();
      if constexpr (s < 9) {
#pragma unroll
        for (int ct = 0; ct < 5; ++ct) wf[(s + 1) & 1][ct] = trread2((s == 8 ? wl : wb + (s + 1) * 32 * 160) + ct * 32, 8 * 160);
      }
#pragma unroll
      for (int ct = 0; ct < 5; ++ct) acc[ct] = MFMA16(wf[s & 1][ct], asbf(of[s]), acc[ct]);
      SGB(0x8, 1); SGB(0x100, 2); SGB(0x8, 1); SGB(0x100, 2); SGB(0x8, 1); SGB(0x100, 2); SGB(0x8, 1); SGB(0x100, 2); SGB(0x8, 1); SGB(0x100, 2);
#pragma unroll
      for (int ct = 0; ct < 5; ++ct) PIN(acc[ct]);
      SB();
    });
    STAMP(10);
    v2u xr[5];
    if (kh == 1) {
#pragma unroll
      for (int ct = 0; ct < 5; ++ct) *(LAS f32x4*)(lds + L_RED + (rt * 5 + ct) * 1024 + lane * 16) = acc[ct];
    } else {
#pragma unroll
      for (int ct = 0; ct < 5; ++ct) { xr[ct] = *(const LAS v2u*)(lds + L_XT + (16 * rt + tk) * 160 + ct * 32 + g4 * 8); PIN(xr[ct]); }
    }
    LBAR();
    STAMP(11);
    DELAY_AT(5);
    if (kh == 0) {
      f32x4 o2[5];
#pragma unroll
      for (int ct = 0; ct < 5; ++ct) o2[ct] = *(const LAS f32x4*)(lds + L_RED + (rt * 5 + ct) * 1024 + lane * 16);
      float s1 = 0.f, s2 = 0.f, s3 = 0.f;
#pragma unroll
      for (int ct = 0; ct < 5; ++ct) {
        const unsigned a01 = pk2(acc[ct][0] + o2[ct][0], acc[ct][1] + o2[ct][1]), a23 = pk2(acc[ct][2] + o2[ct][2], acc[ct][3] + o2[ct][3]);
        hp2[ct][0] = san(pk2(lo16(xr[ct].x) + lo16(a01), hi16(xr[ct].x) + hi16(a01)));
        hp2[ct][1] = san(pk2(lo16(xr[ct].y) + lo16(a23), hi16(xr[ct].y) + hi16(a23)));
        const float h0 = lo16(hp2[ct][0]), h1 = hi16(hp2[ct][0]), h2 = lo16(hp2[ct][1]), h3 = hi16(hp2[ct][1]);
        s1 += (h0 + h1) + (h2 + h3);
        s2 = fmaf(h0, h0, fmaf(h1, h1, fmaf(h2, h2, fmaf(h3, h3, s2))));
        s3 = fmaf(h0, lo16(wr4[ct].x), fmaf(h1, hi16(wr4[ct].x), fmaf(h2, lo16(wr4[ct].y), fmaf(h3, hi16(wr4[ct].y), s3))));
      }
      {
        auto a = __builtin_amdgcn_permlane32_swap(__float_as_uint(s1), __float_as_uint(s2), false, false);
        auto b = __builtin_amdgcn_permlane32_swap(__float_as_uint(s3), 0u, false, false);
        const float A = __uint_as_float(a[0]) + __uint_as_float(a[1]), B = __uint_as_float(b[0]) + __uint_as_float(b[1]);
        auto c = __builtin_amdgcn_permlane16_swap(__float_as_uint(A), __float_as_uint(B), false, false);
        const float tot = __uint_as_float(c[0]) + __uint_as_float(c[1]);
        st32(rs, WS_S + (rb * 8 + mm) * 1024 + (16 * rt + tk) * 16 + ((g4 & 1) * 2 + (g4 >> 1) + (g4 == 3 ? 0 : 0)) * 4, san(__float_as_uint(tot)));
      }
#pragma unroll
      for (int ct = 0; ct < 5; ++ct) st64(rs, WS_H + ((rb * 8 + mm) * 5 + ct) * 2048 + (16 * rt + tk) * 32 + g4 * 8, v2u{hp2[ct][0], hp2[ct][1]});
      STAMP(12);
    }
    if (!leader) BAR();
  }

  if (wave >= 5) {
    DELAY_AT(3);
    if (is_gu) {
      const unsigned voff = (lane >> 1) * 3456 + (lane & 1) * 16;
#pragma unroll 1
      for (int n = wave - 5; n < 40; n += 3) {
        const int mat = n >= 20, blk = n - 20 * mat;
        dma16_u<L_FW>(voff, uni((const GAS unsigned char*)(mat ? P_wu : P_wg) + fsl * 32 + (size_t)blk * 32 * 3456), lds0 + n * 1024);
      }
    } else if (is_dn) {
      const unsigned voff = (lane >> 1) * 1280 + (lane & 1) * 16;
#pragma unroll 1
      for (int n = wave - 5; n < 54; n += 3) dma16_u<L_FW>(voff, uni((const GAS unsigned char*)P_wd + dsl * 32 + (size_t)n * 32 * 1280), lds0 + n * 1024);
    }
  }
  float mb, sd, s3;
  auto gather_stats = [&]() {
    v4u sp4[8];
    for (unsigned sp = 0; sp < SPIN_CAP; ++sp) {
      CBAR();
      unsigned mx = 0;
#pragma unroll
      for (int k = 0; k < 8; ++k) sp4[k] = ld128s(rs, WS_S + lane * 16 + k * 1024, rb * 8192);
#pragma unroll
      for (int k = 0; k < 8; ++k) mx = umax3(umax3(mx, sp4[k].x, sp4[k].y), sp4[k].z, 0u);
      if (__all(mx != SENT)) break;
    }
    float s1 = 0.f, s2 = 0.f; s3 = 0.f;
#pragma unroll
    for (int k = 0; k < 8; ++k) { s1 += __uint_as_float(sp4[k].x); s2 += __uint_as_float(sp4[k].y); s3 += __uint_as_float(sp4[k].z); }
    const float mean = s1 * (1.f / 640.f), var = fmaxf(s2 * (1.f / 640.f) - mean * mean, 0.f);
    mb = rbf(mean); sd = rbf(__builtin_amdgcn_sqrtf(rbf(rbf(var) + 1e-5f)));
    *(LAS f32x2*)(lds + L_ST + lane * 8) = f32x2{mb, rcp(sd)};
  };
  if (wave == 4) {
    if (leader) {
      gather_stats();
      STAMP(13);
      const unsigned rbits = pk2((s3 - mb * wsum) * rcp(sd), 0.f) & 0xFFFFu;
      const unsigned ord = (rbits & 0x8000u) ? (~rbits & 0xFFFFu) : (rbits | 0x8000u);
      unsigned key = (ord << 16) | (2047u - (64 * rb + lane));
      st64(rs, WS_RK + rb * 1024 + 256 + lane * 8, v2u{(__float_as_uint(mb) >> 16) | (__float_as_uint(sd) & 0xFFFF0000u), tlo});
      unsigned top = 0;
#pragma unroll
      for (int i = 0; i < 8; ++i) {
        unsigned mx = key;
        mx = umax(mx, dppu<0x128>(mx)); mx = umax(mx, dppu<0x124>(mx)); mx = umax(mx, dppu<0x122>(mx)); mx = umax(mx, dppu<0x121>(mx));
        const unsigned best = umax(umax((unsigned)__builtin_amdgcn_readlane((int)mx, 0), (unsigned)__builtin_amdgcn_readlane((int)mx, 16)),
                                   umax((unsigned)__builtin_amdgcn_readlane((int)mx, 32), (unsigned)__builtin_amdgcn_readlane((int)mx, 48)));
        top = lane == i ? best : top;
        key = key == best ? 0u : key;
      }
      if (lane < 8) st64(rs, WS_RK + rb * 1024 + lane * 8, v2u{top, tlo});
      STAMP(14);
    }
    v4u kq4[4];
    for (unsigned sp = 0; sp < SPIN_CAP; ++sp) {
      CBAR();
#pragma unroll
      for (int j = 0; j < 4; ++j) kq4[j] = ld128(rs, WS_RK + t * 1024 + j * 16);
      bool ok = true;
#pragma unroll
      for (int j = 0; j < 4; ++j) ok &= kq4[j].y == tlo && kq4[j].w == tlo;
      if (__all(ok)) break;
      __builtin_amdgcn_s_sleep(1);
    }
    STAMP(15);
    unsigned k[8] = {kq4[0].x, kq4[0].z, kq4[1].x, kq4[1].z, kq4[2].x, kq4[2].z, kq4[3].x, kq4[3].z}, selv = 0;
#pragma unroll
    for (int i = 0; i < 8; ++i) {
      unsigned mx = k[0];
      mx = umax(mx, dppu<0x128>(mx)); mx = umax(mx, dppu<0x124>(mx)); mx = umax(mx, dppu<0x122>(mx)); mx = umax(mx, dppu<0x121>(mx));
      const unsigned best = umax((unsigned)__builtin_amdgcn_readlane((int)mx, 0), (unsigned)__builtin_amdgcn_readlane((int)mx, 16));
      selv = lane == i ? best : selv;
      const bool win = k[0] == best;
#pragma unroll
      for (int jx = 0; jx < 7 - i; ++jx) k[jx] = win ? k[jx + 1] : k[jx];
    }
    if (lane < 8) *(LAS unsigned*)(lds + L_SEL + lane * 4) = selv;
    if (wg == 0 && lane == 0) st64(rs, WS_EPOCH, v2u{tlo, thi});
  }
  unsigned hoff[5];
#pragma unroll
  for (int s = 0; s < 5; ++s) { const int col = 32 * (5 * (wave >> 1) + s) + 8 * g4, m8 = col / 80, cr = col - 80 * m8; hoff[s] = (m8 * 5 + (cr >> 4)) * 2048 + (cr & 8) * 2; PIN(hoff[s]); }
  if (wave >= 5) WAITV(0);
  LBAR();
  STAMP(16);
  DELAY_AT(4);
  const rsrc_t ro = __builtin_amdgcn_make_buffer_rsrc((void*)P_out, 0, 2048 * 1280, 0x00020000);
  const unsigned mykey = *(const LAS unsigned*)(lds + L_SEL + (lane & 7) * 4);
  const int mytok = 2047 - (int)(mykey & 0xFFFFu);
  const unsigned hrow = WS_H + (mytok >> 6) * 8 * 5 * 2048 + (mytok & 63) * 32, msda = WS_RK + (mytok >> 6) * 1024 + 256 + (mytok & 63) * 8;

  if (is_gu) {
    const int mat = wave & 1, kq = wave >> 1;
    v4u hx[5]; v2u msd; unsigned ha[5];
    bf16x8 wfr[5];
    {
      lptr wb = lds + L_FW + mat * 20480 + (kq * 160 + 8 * g4 + (tk >> 2)) * 32 + (tk & 3) * 8;
#pragma unroll
      for (int s = 0; s < 5; ++s) wfr[s] = trread2(wb + s * 32 * 32, 4 * 32);
    }
#pragma unroll
    for (int s = 0; s < 5; ++s) ha[s] = hrow + hoff[s];
    for (unsigned sp = 0; sp < SPIN_CAP; ++sp) {
      CBAR();
      msd = ld64(rs, msda);
#pragma unroll
      for (int s = 0; s < 5; ++s) hx[s] = ld128(rs, ha[s]);
      unsigned mx = 0;
#pragma unroll
      for (int s = 0; s < 5; ++s) mx = umax3(umax3(mx, hx[s].x, hx[s].y), hx[s].z, hx[s].w);
      if (__all(mx != SENT && msd.y == tlo)) break;
    }
    const float mb = lo16(msd.x), rsd = rcp(hi16(msd.x)), nmr = -mb * rsd;
    v4u xn[5];
#pragma unroll
    for (int s = 0; s < 5; ++s)
#pragma unroll
      for (int e = 0; e < 4; ++e) { const f32x2 v_ = f32x2{lo16(hx[s][e]), hi16(hx[s][e])} * f32x2{rsd, rsd} + f32x2{nmr, nmr}; xn[s][e] = pk2(v_[0], v_[1]); }
    STAMP(22);
    f32x4 acc = {0, 0, 0, 0};
#pragma unroll
    for (int s = 0; s < 5; ++s) acc = MFMA16(wfr[s], asbf(xn[s]), acc);
    *(LAS f32x4*)(lds + L_FRED + wave * 1024 + lane * 16) = acc;
    LBAR();
    if (wave == 4) {
      f32x4 gs = {0, 0, 0, 0}, us = {0, 0, 0, 0};
#pragma unroll
      for (int k = 0; k < 4; ++k) { gs += *(const LAS f32x4*)(lds + L_FRED + (2 * k) * 1024 + lane * 16); us += *(const LAS f32x4*)(lds + L_FRED + (2 * k + 1) * 1024 + lane * 16); }
      float av[4];
#pragma unroll
      for (int e = 0; e < 4; ++e) { const float gg = rbf(gs[e]), uu = rbf(us[e]); av[e] = rbf(gg * rcp(1.f + __builtin_amdgcn_exp2f(gg * -1.4426950408889634f))) * uu; }
      if (tk < 8) st64(rs, WS_ACT + (fsl >> 1) * 512 + ((fsl & 1) * 2 + (g4 >> 1)) * 128 + tk * 16 + (g4 & 1) * 8, v2u{san(pk2(av[0], av[1])), san(pk2(av[2], av[3]))});
      STAMP(17);
    }
  }
  if (!leader) { if (wave == 0) gather_stats(); LBAR(); }
  v4u af[7]; v2u msd2 = {0, 0}, hv = {0, 0};
  unsigned hva = 0;
  bf16x8 wdn[7];
  if (is_dn) {
    lptr wb = lds + L_FW + (8 * g4 + (tk >> 2)) * 32 + (tk & 3) * 8;
#pragma unroll
    for (int i = 0; i < 7; ++i) wdn[i] = trread2(wb + min(wave + 8 * i, 53) * 1024, 4 * 32);
    const int col = 16 * dsl + 4 * g4, m8 = col / 80, cr = col - 80 * m8;
    hva = hrow + (m8 * 5 + (cr >> 4)) * 2048 + (cr & 15) * 2;
    msd2 = ld64(rs, msda); hv = ld64(rs, hva);
  }
  if (wave < 4) {
    const int tok = 64 * rb + 16 * rt + tk;
    const v4u sa = *(const LAS v4u*)(lds + L_SEL), sb = *(const LAS v4u*)(lds + L_SEL + 16);
    const unsigned me = 2047u - tok;
    const bool routed = (sa.x & 0xFFFFu) == me || (sa.y & 0xFFFFu) == me || (sa.z & 0xFFFFu) == me || (sa.w & 0xFFFFu) == me ||
                        (sb.x & 0xFFFFu) == me || (sb.y & 0xFFFFu) == me || (sb.z & 0xFFFFu) == me || (sb.w & 0xFFFFu) == me;
    const f32x2 st = *(const LAS f32x2*)(lds + L_ST + (16 * rt + tk) * 8);
    if (!routed) {
#pragma unroll
      for (int ct = 0; ct < 5; ++ct) {
        const float h0 = lo16(hp2[ct][0]), h1 = hi16(hp2[ct][0]), h2 = lo16(hp2[ct][1]), h3 = hi16(hp2[ct][1]);
        const unsigned d01 = pk2(h0 - st[0], h1 - st[0]), d23 = pk2(h2 - st[0], h3 - st[0]);
        const unsigned n01 = pk2(lo16(d01) * st[1], hi16(d01) * st[1]), n23 = pk2(lo16(d23) * st[1], hi16(d23) * st[1]);
        st64(ro, tok * 1280 + (80 * mm + 16 * ct + 4 * g4) * 2, v2u{pk2(h0 + lo16(n01), h1 + hi16(n01)), pk2(h2 + lo16(n23), h3 + hi16(n23))});
      }
    }
  }
  if (is_dn) {
    for (unsigned sp = 0; sp < SPIN_CAP; ++sp) { if (__all(msd2.y == tlo && hv.x != SENT && hv.y != SENT)) break; CBAR(); msd2 = ld64(rs, msda); hv = ld64(rs, hva); }
    float hh[4], nn[4], sg;
    {
      const unsigned ord = mykey >> 16, rbits = (ord & 0x8000u) ? (ord & 0x7FFFu) : (~ord & 0xFFFFu);
      const float r = __uint_as_float(rbits << 16), mb = lo16(msd2.x), rsd = rcp(hi16(msd2.x));
      sg = rbf(rcp(1.f + __builtin_amdgcn_exp2f(r * -1.4426950408889634f)));
      hh[0] = lo16(hv.x); hh[1] = hi16(hv.x); hh[2] = lo16(hv.y); hh[3] = hi16(hv.y);
#pragma unroll
      for (int e = 0; e < 4; ++e) { nn[e] = rbf(rbf(hh[e] - mb) * rsd); PIN(nn[e]); }
      PIN(sg);
    }
    for (unsigned sp = 0; sp < SPIN_CAP; ++sp) {
      CBAR();
      unsigned mx = 0;
#pragma unroll
      for (int i = 0; i < 7; ++i) af[i] = ld128(rs, WS_ACT + min(wave + 8 * i, 53) * 512 + g4 * 128 + (tk & 7) * 16);
#pragma unroll
      for (int i = 0; i < 7; ++i) mx = umax3(umax3(mx, af[i].x, af[i].y), af[i].z, af[i].w);
      if (__all(mx != SENT)) break;
      __builtin_amdgcn_s_sleep(1);
    }
    STAMP(18);
    f32x4 acc = {0, 0, 0, 0};
#pragma unroll
    for (int i = 0; i < 7; ++i) {
      if (wave + 8 * i < 54) acc = MFMA16(wdn[i], asbf(af[i]), acc);
    }
    *(LAS f32x4*)(lds + L_FRED + wave * 1024 + lane * 16) = acc;
    LBAR();
    if (wave == 4 && tk < 8) {
      f32x4 y = {0, 0, 0, 0};
#pragma unroll
      for (int k = 0; k < 8; ++k) y += *(const LAS f32x4*)(lds + L_FRED + k * 1024 + lane * 16);
      float o4[4];
#pragma unroll
      for (int e = 0; e < 4; ++e) o4[e] = hh[e] + rbf(nn[e] + rbf(rbf(y[e]) * sg));
      st64(ro, mytok * 1280 + (16 * dsl + 4 * g4) * 2, v2u{pk2(o4[0], o4[1]), pk2(o4[2], o4[3])});
    }
  }
  STAMP(19);
  STAMP_DUMP();
}

extern "C" void kernel_launch(void* const* d_in, const int* in_sizes, int n_in, void* d_out, int out_size, void* d_ws, size_t ws_size, hipStream_t stream) {
  (void)in_sizes; (void)n_in; (void)out_size; (void)ws_size;
  Params p;
  p.x = (const u16*)d_in[0]; p.wqkv = (const u16*)d_in[1]; p.wo = (const u16*)d_in[2];
  p.wr = (const u16*)d_in[4]; p.wg = (const u16*)d_in[5]; p.wu = (const u16*)d_in[6]; p.wd = (const u16*)d_in[7];
  p.out = (u16*)d_out; p.ws = (unsigned char*)d_ws;
  (void)hipFuncSetAttribute((const void*)mega_kernel, hipFuncAttributeMaxDynamicSharedMemorySize, LDS_BYTES);
  mega_kernel<<<256, 512, LDS_BYTES, stream>>>(p);
}
```
